```python
import torch

def _softmax(x, axis=-1):
    return torch.softmax(x, dim=axis)
def _gelu(x):
    return torch.nn.functional.gelu(x, approximate="tanh")
def _silu(x):
    return torch.nn.functional.silu(x)
def _sigmoid(x):
    return torch.sigmoid(x)
def _layernorm(x, axis=-1, eps=1e-5):
    m = x.mean(dim=axis, keepdim=True)
    v = x.var(dim=axis, keepdim=True, unbiased=False)
    return (x - m) / torch.sqrt(v + eps)
def _scatter_add(tgt, idx, src, axis=0):
    return tgt.clone().index_add_(axis, idx, src)
def _segment_sum(src, seg, n):
    out = torch.zeros((n,) + src.shape[1:], dtype=src.dtype)
    return out.index_add_(0, seg, src)
def _pad(x, pw):
    flat = tuple(v for pair in reversed(pw) for v in pair)
    return torch.nn.functional.pad(x, flat)

kernel_name = 'gated_mix__grouped_kv_mqa__spectral_fnet__prompt'

def setup_inputs():

    _w = (lambda a: (lambda t: t.to(torch.bfloat16) if t.is_floating_point() else t)(torch.as_tensor(a)))
    def _A_setup():
        B, S, D, H = 1, 2048, 640, 32
        Dh = D // H
        return {
            "x": _w((lambda *s: torch.randn(*s, dtype=torch.bfloat16) * 0.05)(B, S, D)),
            "wq": _w((lambda *s: torch.randn(*s, dtype=torch.bfloat16) * 1.6)(D, H * Dh)),
            "wk": _w((lambda *s: torch.randn(*s, dtype=torch.bfloat16) * 1.6)(D, Dh)),
            "wv": _w((lambda *s: torch.randn(*s, dtype=torch.bfloat16) * 1.6)(D, Dh)),
            "wo": _w((lambda *s: torch.randn(*s, dtype=torch.bfloat16) * 1.6)(H * Dh, D)),
        }
    _A_in = _A_setup()

    def _B_setup():
        B, S, D = 1, 2048, 640
        ns = torch.arange(S)[:, None] * torch.arange(S)[None, :]
        nd = torch.arange(D)[:, None] * torch.arange(D)[None, :]
        Ws_re = torch.cos(-2.0 * torch.pi * ns / S).to(float)
        Ws_im = torch.sin(-2.0 * torch.pi * ns / S).to(float)
        Wd_re = torch.cos(-2.0 * torch.pi * nd / D).to(float)
        Wd_im = torch.sin(-2.0 * torch.pi * nd / D).to(float)
        return {
            "x": _w((lambda *s: torch.randn(*s, dtype=torch.bfloat16) * 0.05)(B, S, D)),
            "dft_seq_re": _w(Ws_re),
            "dft_seq_im": _w(Ws_im),
            "dft_hid_re": _w(Wd_re),
            "dft_hid_im": _w(Wd_im),
        }
    _B_in = _B_setup()

    return {
        "A_x": _A_in["x"],
        "A_wq": _A_in["wq"],
        "A_wk": _A_in["wk"],
        "A_wv": _A_in["wv"],
        "A_wo": _A_in["wo"],
        "B_x": _B_in["x"],
        "B_dft_seq_re": _B_in["dft_seq_re"],
        "B_dft_seq_im": _B_in["dft_seq_im"],
        "B_dft_hid_re": _B_in["dft_hid_re"],
        "B_dft_hid_im": _B_in["dft_hid_im"],
        "gate_w": _w((lambda *s: torch.randn(*s, dtype=torch.bfloat16) * 0.05)(640) * 16.0),
    }

def reference(A_x, A_wq, A_wk, A_wv, A_wo, B_x, B_dft_seq_re, B_dft_seq_im, B_dft_hid_re, B_dft_hid_im, gate_w):
    def _A_ref(x, wq, wk, wv, wo):
        B, S, D = x.shape
        H = 32
        Dh = D // H
        q = (lambda x, *d: x.permute(*d))((x @ wq).reshape(B, S, H, Dh), 0, 2, 1, 3)
        k = x @ wk
        v = x @ wv
        scores = torch.einsum("bhqd,bkd->bhqk", q, k) * (Dh ** -0.5)
        p = _softmax(scores, axis=-1)
        o = torch.einsum("bhqk,bkd->bhqd", p, v)
        return (lambda x, *d: x.permute(*d))(o, 0, 2, 1, 3).reshape(B, S, H * Dh) @ wo

    def _B_ref(x, dft_seq_re, dft_seq_im, dft_hid_re, dft_hid_im):
        a = torch.einsum("bsd,ed->bse", x, dft_hid_re)
        b = torch.einsum("bsd,ed->bse", x, dft_hid_im)
        y = torch.einsum("ts,bse->bte", dft_seq_re, a) - torch.einsum("ts,bse->bte", dft_seq_im, b)
        return y

    x = A_x
    g = _sigmoid((x * gate_w).sum(-1, keepdims=True))
    a = _A_ref(x, A_wq, A_wk, A_wv, A_wo)
    b = _B_ref(x, B_dft_seq_re, B_dft_seq_im, B_dft_hid_re, B_dft_hid_im)
    return g * a + (1 - g) * b
```

```cpp
#include <hip/hip_runtime.h>
#include <hip/hip_bf16.h>
#include <stdint.h>

typedef __bf16 bf16x8 __attribute__((ext_vector_type(8)));
typedef __bf16 bf16x4 __attribute__((ext_vector_type(4)));
typedef __bf16 bf16x2 __attribute__((ext_vector_type(2)));
typedef short s16x4 __attribute__((ext_vector_type(4)));
typedef float f32x16 __attribute__((ext_vector_type(16)));
typedef float f32x4 __attribute__((ext_vector_type(4)));
typedef float f32x2 __attribute__((ext_vector_type(2)));
typedef unsigned u32x4 __attribute__((ext_vector_type(4)));
typedef unsigned u32x2 __attribute__((ext_vector_type(2)));
typedef unsigned short u16;
#define LDSP __attribute__((address_space(3)))
#define DI __device__ __forceinline__
#define MFMA32(a, b, c) __builtin_amdgcn_mfma_f32_32x32x16_bf16((a), (b), (c), 0, 0, 0)
#define MFMA16(a, b, c) __builtin_amdgcn_mfma_f32_16x16x32_bf16((a), (b), (c), 0, 0, 0)
#define SBAR() __builtin_amdgcn_sched_barrier(0)

constexpr int S = 2048, D = 640, NH = 32, DH = 20;
constexpr float QSCALE = 0.22360679774997896f * 1.4426950408889634f;

constexpr size_t WS_QP = 0;
constexpr size_t WS_KV = WS_QP + (size_t)S * D * 2;
constexpr int KV_STAGE = 12288, KV_TILE = 2880;
constexpr size_t WS_BT = WS_KV + 16 * KV_STAGE;
constexpr size_t WS_WF = WS_BT + (size_t)1280 * S * 2;
constexpr size_t WS_G = WS_WF + (size_t)D * D * 2;
constexpr size_t WS_KM = WS_G + S * 4;
constexpr size_t WS_OF = WS_KM + 256;
constexpr size_t WS_YP = WS_OF + (size_t)S * D * 2;
constexpr size_t WS_END = WS_YP + (size_t)4 * S * D * 2;

DI unsigned pk2(float a, float b) { f32x2 v = {a, b}; bf16x2 r = __builtin_convertvector(v, bf16x2); return __builtin_bit_cast(unsigned, r); }
DI float bf2f(u16 x) { return __uint_as_float((unsigned)x << 16); }
DI __amdgpu_buffer_rsrc_t mk_rsrc(const void* p, unsigned bytes) { return __builtin_amdgcn_make_buffer_rsrc((void*)p, (short)0, (int)bytes, 0x00020000); }
DI void dma16(__amdgpu_buffer_rsrc_t rs, unsigned voff, unsigned soff, unsigned lds) {
    asm volatile("s_mov_b32 m0, %3\n\ts_nop 0\n\tbuffer_load_dwordx4 %0, %1, %2 offen lds" ::"v"(voff), "s"(rs), "s"(soff), "s"(lds) : "memory");
}
#define WAIT_VM(n) asm volatile("s_waitcnt vmcnt(" #n ")" ::: "memory")
#define WAIT_LGKM0() asm volatile("s_waitcnt lgkmcnt(0)" ::: "memory")
#define BARRIER() asm volatile("s_barrier" ::: "memory")
DI bf16x8 lds_ld8(unsigned a) { return *(const LDSP bf16x8*)(uintptr_t)a; }
DI s16x4 lds_tr(unsigned a) { return __builtin_amdgcn_ds_read_tr16_b64_v4i16((LDSP s16x4*)(uintptr_t)a); }

constexpr int K1_STAGE = 32768, K1_NS = 4, K1_LDS = K1_STAGE * K1_NS;
struct K1Args {
    const u16 *xa, *wq, *wk, *wv, *wo, *xb, *wr, *wi, *gw;
    char* ws;
};

template <int MODE>
DI void k1_gemm(f32x16 (&acc)[2][2], unsigned lds0, __amdgpu_buffer_rsrc_t rx, unsigned xrow0, __amdgpu_buffer_rsrc_t rw, unsigned wbase, int wid, int lane) {
    const unsigned xr = lane >> 3, xc = lane & 7;
    const unsigned xv0 = (32 * wid + xr) * 1280 + ((xc ^ ((xr >> 1) & 7)) << 4);
    const unsigned xv1 = (32 * wid + xr) * 1280 + ((xc ^ ((4 + (xr >> 1)) & 7)) << 4);
    unsigned wv0, wv1;
    if (MODE == 0) {
        const unsigned kr = lane >> 4, cc = lane & 15;
        wv0 = (16 * wid + kr) * 1280 + (((((cc >> 2) ^ kr) << 2) | (cc & 3)) << 4);
        wv1 = wv0;
    } else {
        wv0 = xv0; wv1 = xv1;
    }
    auto issue = [&](int st) {
        const unsigned sl = lds0 + (st & (K1_NS - 1)) * K1_STAGE;
        const unsigned xs = xrow0 * 1280 + st * 128;
#pragma unroll
        for (int p = 0; p < 4; ++p) dma16(rx, (p & 1) ? xv1 : xv0, xs + p * 8 * 1280, sl + (wid * 4 + p) * 1024);
        if (MODE == 0) {
            const unsigned wsb = wbase + st * 64 * 1280;
#pragma unroll
            for (int p = 0; p < 4; ++p) dma16(rw, wv0, wsb + p * 4 * 1280, sl + 16384 + (wid * 4 + p) * 1024);
        } else {
            const unsigned wsb = wbase + st * 128;
#pragma unroll
            for (int p = 0; p < 4; ++p) dma16(rw, (p & 1) ? wv1 : wv0, wsb + p * 8 * 1280, sl + 16384 + (wid * 4 + p) * 1024);
        }
    };
    issue(0); issue(1); issue(2);
    const int r = lane & 31, h = lane >> 5;
    const int wi = wid >> 1, wj = wid & 1;
    const unsigned rowpart = r * 128;
    const unsigned sw = (r >> 1) & 7;
    const unsigned tq = (lane & 15) >> 2;
    const unsigned trpart = (8 * h + tq) * 256 + ((lane >> 4) & 1) * 32 + (lane & 3) * 8;
#pragma unroll 1
    for (int st = 0; st < 10; ++st) {
        if (st < 8) { WAIT_VM(16); } else if (st == 8) { WAIT_VM(8); } else { WAIT_VM(0); }
        WAIT_LGKM0();
        BARRIER();
        if (st + 3 < 10) issue(st + 3);
        const unsigned sl = lds0 + (st & (K1_NS - 1)) * K1_STAGE;
#pragma unroll
        for (int ks = 0; ks < 4; ++ks) {
            bf16x8 xf[2], wf[2];
            const int xw = (MODE == 0) ? wj : wi;
            const int ww = (MODE == 0) ? wi : wj;
#pragma unroll
            for (int b = 0; b < 2; ++b) xf[b] = lds_ld8(sl + (xw * 64 + b * 32) * 128 + rowpart + (((2 * ks + h) ^ sw) << 4));
            if (MODE == 0) {
#pragma unroll
                for (int b = 0; b < 2; ++b) {
                    const unsigned G = ww * 2 + b;
                    const unsigned a0 = sl + 16384 + ks * 16 * 256 + trpart + ((G ^ tq) << 6);
                    s16x4 lo = lds_tr(a0), hi = lds_tr(a0 + 1024);
                    wf[b] = __builtin_bit_cast(bf16x8, __builtin_shufflevector(lo, hi, 0, 1, 2, 3, 4, 5, 6, 7));
                }
            } else {
#pragma unroll
                for (int b = 0; b < 2; ++b) wf[b] = lds_ld8(sl + 16384 + (ww * 64 + b * 32) * 128 + rowpart + (((2 * ks + h) ^ sw) << 4));
            }
#pragma unroll
            for (int bi = 0; bi < 2; ++bi)
#pragma unroll
                for (int bj = 0; bj < 2; ++bj)
                    acc[bi][bj] = (MODE == 0) ? MFMA32(wf[bi], xf[bj], acc[bi][bj]) : MFMA32(xf[bi], wf[bj], acc[bi][bj]);
        }
    }
    WAIT_LGKM0();
    BARRIER();
}

DI void k1_store_tile(const f32x16 (&acc)[2][2], float scale, char* lds, u16* dst, int pitch_elems, int tid, int wid, int lane) {
    const int r = lane & 31, h = lane >> 5, wi = wid >> 1, wj = wid & 1;
#pragma unroll
    for (int bi = 0; bi < 2; ++bi)
#pragma unroll
        for (int bj = 0; bj < 2; ++bj)
#pragma unroll
            for (int g = 0; g < 4; ++g) {
                u32x2 v; v[0] = pk2(acc[bi][bj][4 * g] * scale, acc[bi][bj][4 * g + 1] * scale); v[1] = pk2(acc[bi][bj][4 * g + 2] * scale, acc[bi][bj][4 * g + 3] * scale);
                const int row = wj * 64 + bj * 32 + r, col = wi * 64 + bi * 32 + 8 * g + 4 * h;
                *(u32x2*)(lds + row * 272 + col * 2) = v;
            }
    __syncthreads();
#pragma unroll
    for (int p = 0; p < 8; ++p) {
        const int row = p * 16 + (tid >> 4), ch = tid & 15;
        const u32x4 v = *(const u32x4*)(lds + row * 272 + ch * 16);
        *(u32x4*)(dst + (size_t)row * pitch_elems + ch * 8) = v;
    }
}

__global__ __launch_bounds__(256) void k1_proj(K1Args a) {
    extern __shared__ __attribute__((aligned(16))) char lds[];
    const int tid = threadIdx.x, lane = tid & 63, wid = __builtin_amdgcn_readfirstlane(tid >> 6);
    const int xcd = blockIdx.x & 7, idx = blockIdx.x >> 3;
    const int mb = 2 * xcd + (idx >> 4), t = idx & 15;
    const unsigned lds0 = (unsigned)(uintptr_t)(LDSP char*)lds;
    f32x16 acc[2][2];
#pragma unroll
    for (int i = 0; i < 2; ++i)
#pragma unroll
        for (int j = 0; j < 2; ++j)
#pragma unroll
            for (int k = 0; k < 16; ++k) acc[i][j][k] = 0.f;
    if (t < 5) {
        k1_gemm<0>(acc, lds0, mk_rsrc(a.xa, S * D * 2), mb * 128, mk_rsrc(a.wq, D * D * 2), t * 256, wid, lane);
        k1_store_tile(acc, QSCALE, lds, (u16*)(a.ws + WS_QP) + (size_t)mb * 128 * D + t * 128, D, tid, wid, lane);
    } else if (t >= 6) {
        const int eb = t - 6;
        const int pair = mb * 10 + eb, nt = pair / 20, step = pair % 20;
        u16 wv[2][8];
#pragma unroll
        for (int c = 0; c < 2; ++c) {
            const int ch = tid + c * 256;
            const int blk = ch >> 6, l = ch & 63;
            const int e = 80 * nt + 16 * (blk < 5 ? blk : 0) + (l & 15), k0 = 32 * step + 8 * (l >> 4);
#pragma unroll
            for (int j = 0; j < 8; ++j) wv[c][j] = a.wo[(size_t)(k0 + j) * D + e];
        }
        const u16* w = eb < 5 ? a.wr : a.wi;
        k1_gemm<1>(acc, lds0, mk_rsrc(a.xb, S * D * 2), mb * 128, mk_rsrc(w, D * D * 2), (eb < 5 ? eb : eb - 5) * 128 * 1280, wid, lane);
        k1_store_tile(acc, eb < 5 ? 1.f : -1.f, lds, (u16*)(a.ws + WS_BT) + (size_t)eb * 128 * S + mb * 128, S, tid, wid, lane);
#pragma unroll
        for (int c = 0; c < 2; ++c) {
            const int ch = tid + c * 256;
            if (ch < 320) {
                u32x4 v;
#pragma unroll
                for (int j = 0; j < 4; ++j) v[j] = (unsigned)wv[c][2 * j] | ((unsigned)wv[c][2 * j + 1] << 16);
                *(u32x4*)(a.ws + WS_WF + ((size_t)(nt * 5 + (ch >> 6)) * 20 + step) * 1024 + (ch & 63) * 16) = v;
            }
        }
    } else {
        char* wimg = lds;
        for (int i = tid; i < 640 * 10; i += 256) {
            const int k = i / 10, c = i % 10;
            const u32x2 v = *(const u32x2*)((c < 5 ? a.wk : a.wv) + k * 20 + (c < 5 ? c : c - 5) * 4);
            const int byte = c * 8;
            const int half = (byte >> 6) ^ ((k >> 1) & 1);
            *(u32x2*)(wimg + k * 128 + half * 64 + (byte & 63)) = v;
        }
        for (int k = tid; k < 640; k += 256) {
            const int half = 1 ^ ((k >> 1) & 1);
            *(u16*)(wimg + k * 128 + half * 64 + 16) = a.gw[k];
        }
        const __amdgpu_buffer_rsrc_t rx = mk_rsrc(a.xa, S * D * 2);
        const unsigned xr = lane >> 3, xc = lane & 7;
        const unsigned xv0 = (32 * wid + xr) * 1280 + ((xc ^ ((xr >> 1) & 7)) << 4);
        const unsigned xv1 = (32 * wid + xr) * 1280 + ((xc ^ ((4 + (xr >> 1)) & 7)) << 4);
        const unsigned xl0 = lds0 + 81920;
        auto issue = [&](int st) {
            const unsigned sl = xl0 + (st % 3) * 16384;
#pragma unroll
            for (int p = 0; p < 4; ++p) dma16(rx, (p & 1) ? xv1 : xv0, mb * 128 * 1280 + st * 128 + p * 8 * 1280, sl + (wid * 4 + p) * 1024);
        };
        WAIT_VM(0);
        issue(0); issue(1);
        const int r = lane & 31, h = lane >> 5;
        const unsigned sw = (r >> 1) & 7, tq = (lane & 15) >> 2;
        f32x16 c0 = acc[0][0], c1 = acc[0][1];
#pragma unroll 1
        for (int st = 0; st < 10; ++st) {
            if (st < 9) { WAIT_VM(4); } else { WAIT_VM(0); }
            WAIT_LGKM0();
            BARRIER();
            if (st + 2 < 10) issue(st + 2);
            const unsigned sl = xl0 + (st % 3) * 16384;
#pragma unroll
            for (int ks = 0; ks < 4; ++ks) {
                const bf16x8 xf = lds_ld8(sl + wid * 32 * 128 + r * 128 + (((2 * ks + h) ^ sw) << 4));
                const int k0 = st * 64 + ks * 16 + 8 * h + tq;
                const unsigned s0 = (k0 >> 1) & 1, s1 = ((k0 + 4) >> 1) & 1;
                const unsigned cpart = ((lane >> 4) & 1) * 32 + (lane & 3) * 8;
                bf16x8 wf[2];
#pragma unroll
                for (int b = 0; b < 2; ++b) {
                    s16x4 lo = lds_tr(lds0 + k0 * 128 + ((b ^ s0) << 6) + cpart), hi = lds_tr(lds0 + (k0 + 4) * 128 + ((b ^ s1) << 6) + cpart);
                    wf[b] = __builtin_bit_cast(bf16x8, __builtin_shufflevector(lo, hi, 0, 1, 2, 3, 4, 5, 6, 7));
                }
                c0 = MFMA32(wf[0], xf, c0); c1 = MFMA32(wf[1], xf, c1);
            }
        }
        WAIT_LGKM0();
        BARRIER();
        float* T = (float*)lds;
#pragma unroll
        for (int g = 0; g < 4; ++g) {
            f32x4 v0 = {c0[4 * g], c0[4 * g + 1], c0[4 * g + 2], c0[4 * g + 3]}, v1 = {c1[4 * g], c1[4 * g + 1], c1[4 * g + 2], c1[4 * g + 3]};
            *(f32x4*)(T + (wid * 32 + r) * 68 + 8 * g + 4 * h) = v0;
            *(f32x4*)(T + (wid * 32 + r) * 68 + 32 + 8 * g + 4 * h) = v1;
        }
        __syncthreads();
        char* img = a.ws + WS_KV + (size_t)mb * KV_STAGE;
        for (int i = tid; i < 384; i += 256) {
            const int tt = i / 96, c = (i % 96) >> 5, key = i & 31;
            const float* row = T + (tt * 32 + key) * 68;
            u32x4 v;
            if (c < 2) { v[0] = pk2(row[8 * c], row[8 * c + 1]); v[1] = pk2(row[8 * c + 2], row[8 * c + 3]); v[2] = pk2(row[8 * c + 4], row[8 * c + 5]); v[3] = pk2(row[8 * c + 6], row[8 * c + 7]); }
            else { v[0] = pk2(row[16], row[17]); v[1] = pk2(row[18], row[19]); v[2] = 0x00003F80u; v[3] = 0u; }
            *(u32x4*)(img + tt * KV_TILE + c * 512 + key * 16) = v;
        }
        for (int i = tid; i < 4 * 84; i += 256) {
            const int tt = i / 84, q = i % 84, sh = q / 21, d = q % 21, s2 = sh >> 1, hh = sh & 1;
            float e[8];
#pragma unroll
            for (int j = 0; j < 8; ++j) { const int key = 16 * s2 + 8 * (j >> 2) + 4 * hh + (j & 3); e[j] = d < 20 ? T[(tt * 32 + key) * 68 + 20 + d] : 1.0f; }
            u32x4 v; v[0] = pk2(e[0], e[1]); v[1] = pk2(e[2], e[3]); v[2] = pk2(e[4], e[5]); v[3] = pk2(e[6], e[7]);
            *(u32x4*)(img + tt * KV_TILE + 1536 + q * 16) = v;
        }
        float n2 = 0.f;
        if (tid < 128) {
            const float* row = T + tid * 68;
#pragma unroll
            for (int d = 0; d < 20; ++d) { const float kv = bf2f((u16)(pk2(row[d], 0.f) & 0xffff)); n2 += kv * kv; }
            ((float*)(a.ws + WS_G))[mb * 128 + tid] = 1.0f / (1.0f + __expf(-row[40]));
        }
#pragma unroll
        for (int o = 32; o >= 1; o >>= 1) n2 = fmaxf(n2, __shfl_xor(n2, o));
        __syncthreads();
        float* red = (float*)(lds + 65536);
        if (lane == 0) red[wid] = n2;
        __syncthreads();
        if (tid == 0) ((float*)(a.ws + WS_KM))[mb] = fmaxf(fmaxf(red[0], red[1]), fmaxf(red[2], red[3]));
    }
}

constexpr int K2_A = 0, K2_B = 3 * 16384, K2_KV = K2_B + 3 * 20480, K2_LDS = K2_KV + 3 * KV_STAGE;
struct K2Args {
    const u16 *cre, *cim;
    char* ws;
};

__global__ __launch_bounds__(512) void k2_main(K2Args a) {
    extern __shared__ __attribute__((aligned(16))) char lds[];
    const int tid = threadIdx.x, lane = tid & 63, wid = __builtin_amdgcn_readfirstlane(tid >> 6);
    const int r = lane & 31, h = lane >> 5;
    const unsigned lds0 = (unsigned)(uintptr_t)(LDSP char*)lds;
    const int c = blockIdx.x, xcd = c & 7, idx = c >> 3;
    const int kq = xcd >> 1, mt = (xcd & 1) * 8 + (idx >> 2), nt = idx & 3;
    const int mi = wid & 3, nj = wid >> 2;
    const int rg = wid >> 1, kh = wid & 1;
    const __amdgpu_buffer_rsrc_t rA = mk_rsrc((kq >> 1) ? a.cim : a.cre, S * S * 2);
    const __amdgpu_buffer_rsrc_t rB = mk_rsrc(a.ws + WS_BT, 1280 * S * 2);
    const __amdgpu_buffer_rsrc_t rK = mk_rsrc(a.ws + WS_KV, 16 * KV_STAGE);
    const unsigned xr = lane >> 3, xc = lane & 7;
    const unsigned v0 = xr * 4096 + ((xc ^ ((xr >> 1) & 7)) << 4), v1 = xr * 4096 + ((xc ^ ((4 + (xr >> 1)) & 7)) << 4), vk = lane * 16;
    const unsigned abase = (unsigned)(mt * 128) * 4096 + (kq & 1) * 2048;
    const unsigned bbase = (unsigned)((kq >> 1) * 640 + nt * 160) * 4096 + (kq & 1) * 2048;
    auto issue = [&](int st) {
        const int sl = st % 3;
        const unsigned k0 = st * 128;
        dma16(rA, v0, abase + (2 * wid) * 8 * 4096 + k0, lds0 + K2_A + sl * 16384 + (2 * wid) * 1024);
        dma16(rA, v1, abase + (2 * wid + 1) * 8 * 4096 + k0, lds0 + K2_A + sl * 16384 + (2 * wid + 1) * 1024);
        if (wid < 4) {
#pragma unroll
            for (int p = 0; p < 3; ++p) { const int pc = 3 * wid + p; dma16(rB, (pc & 1) ? v1 : v0, bbase + pc * 8 * 4096 + k0, lds0 + K2_B + sl * 20480 + pc * 1024); }
            dma16(rK, vk, st * KV_STAGE + wid * 1024, lds0 + K2_KV + sl * KV_STAGE + wid * 1024);
        } else {
#pragma unroll
            for (int p = 0; p < 2; ++p) { const int pc = 12 + 2 * (wid - 4) + p; dma16(rB, p ? v1 : v0, bbase + pc * 8 * 4096 + k0, lds0 + K2_B + sl * 20480 + pc * 1024); }
#pragma unroll
            for (int p = 0; p < 2; ++p) { const int pc = 4 + 2 * (wid - 4) + p; dma16(rK, vk, st * KV_STAGE + pc * 1024, lds0 + K2_KV + sl * KV_STAGE + pc * 1024); }
        }
    };
    issue(0); issue(1);
    const u16* qp = (const u16*)(a.ws + WS_QP);
    bf16x8 q1[2], q2[2];
    float km = ((const float*)(a.ws + WS_KM))[lane & 15];
#pragma unroll
    for (int o = 8; o >= 1; o >>= 1) km = fmaxf(km, __shfl_xor(km, o));
#pragma unroll
    for (int cb = 0; cb < 2; ++cb) {
        const int row = c * 256 + rg * 64 + cb * 32 + r;
        const u32x4 f1 = *(const u32x4*)(qp + (size_t)row * 20 + 8 * h);
        const u32x2 f2 = *(const u32x2*)(qp + (size_t)row * 20 + 16);
        float n2 = 0.f;
#pragma unroll
        for (int j = 0; j < 4; ++j) { const float x = __uint_as_float(f1[j] << 16), y = __uint_as_float(f1[j] & 0xffff0000u); n2 += x * x + y * y; }
        if (h == 0) {
#pragma unroll
            for (int j = 0; j < 2; ++j) { const float x = __uint_as_float(f2[j] << 16), y = __uint_as_float(f2[j] & 0xffff0000u); n2 += x * x + y * y; }
        }
        n2 += __shfl_xor(n2, 32);
        const float m = __builtin_sqrtf(n2 * km) * 1.01f - 64.f;
        u32x4 g2; g2[0] = h ? 0u : f2[0]; g2[1] = h ? 0u : f2[1]; g2[2] = h ? 0u : (pk2(-m, 0.f) & 0xffffu); g2[3] = 0u;
        q1[cb] = __builtin_bit_cast(bf16x8, f1); q2[cb] = __builtin_bit_cast(bf16x8, g2);
    }
    f32x16 o[2], acc[3];
#pragma unroll
    for (int k = 0; k < 16; ++k) { o[0][k] = 0.f; o[1][k] = 0.f; acc[0][k] = 0.f; acc[1][k] = 0.f; acc[2][k] = 0.f; }
    const unsigned sw = (r >> 1) & 7;
    const int dcl = r <= 20 ? r : (r < 28 ? 20 : r - 12);
    const f32x16 zero = {};
#pragma unroll 1
    for (int st = 0; st < 16; ++st) {
        if (st + 1 < 16) { WAIT_VM(6); } else { WAIT_VM(0); }
        WAIT_LGKM0();
        BARRIER();
        if (st + 2 < 16) issue(st + 2);
        const int sl = st % 3;
        const unsigned sa = lds0 + K2_A + sl * 16384 + mi * 4096 + r * 128;
        const unsigned sb = lds0 + K2_B + sl * 20480 + nj * 3 * 4096 + r * 128;
        const unsigned sk = lds0 + K2_KV + sl * KV_STAGE + kh * 2 * KV_TILE;
#pragma unroll
        for (int tt = 0; tt < 2; ++tt) {
            const bf16x8 k1 = lds_ld8(sk + tt * KV_TILE + h * 512 + r * 16);
            const bf16x8 k2 = lds_ld8(sk + tt * KV_TILE + 1024 + r * 16);
            const bf16x8 vf0 = lds_ld8(sk + tt * KV_TILE + 1536 + (h * 21 + dcl) * 16);
            const bf16x8 vf1 = lds_ld8(sk + tt * KV_TILE + 1536 + ((2 + h) * 21 + dcl) * 16);
#pragma unroll
            for (int cb = 0; cb < 2; ++cb) {
                f32x16 s = MFMA32(k1, q1[cb], zero);
                s = MFMA32(k2, q2[cb], s);
                u32x4 p0, p1;
#pragma unroll
                for (int j = 0; j < 4; ++j) {
                    p0[j] = pk2(__builtin_amdgcn_exp2f(s[2 * j]), __builtin_amdgcn_exp2f(s[2 * j + 1]));
                    p1[j] = pk2(__builtin_amdgcn_exp2f(s[8 + 2 * j]), __builtin_amdgcn_exp2f(s[8 + 2 * j + 1]));
                }
                o[cb] = MFMA32(vf0, __builtin_bit_cast(bf16x8, p0), o[cb]);
                o[cb] = MFMA32(vf1, __builtin_bit_cast(bf16x8, p1), o[cb]);
                const int ks = tt * 2 + cb;
                const unsigned xo = ((2 * ks + h) ^ sw) << 4;
                const bf16x8 af = lds_ld8(sa + xo);
                acc[0] = MFMA32(lds_ld8(sb + xo), af, acc[0]);
                acc[1] = MFMA32(lds_ld8(sb + 4096 + xo), af, acc[1]);
                if (nj == 0) acc[2] = MFMA32(lds_ld8(sb + 8192 + xo), af, acc[2]);
            }
        }
    }
    {
        char* yp = a.ws + WS_YP + (size_t)kq * S * D * 2;
        const int t = mt * 128 + mi * 32 + r, tb = t >> 4;
        const int nb = nj == 0 ? 3 : 2;
#pragma unroll
        for (int b = 0; b < 3; ++b) {
            if (b < nb) {
                const int e0 = nt * 160 + (nj * 3 + b) * 32;
#pragma unroll
                for (int g = 0; g < 4; ++g) {
                    u32x2 v; v[0] = pk2(acc[b][4 * g], acc[b][4 * g + 1]); v[1] = pk2(acc[b][4 * g + 2], acc[b][4 * g + 3]);
                    const int eb = (e0 >> 4) + (g >> 1);
                    *(u32x2*)(yp + ((size_t)(tb * 40 + eb) * 64 + (t & 15) + 16 * (2 * (g & 1) + h)) * 8) = v;
                }
            }
        }
    }
    WAIT_LGKM0();
    BARRIER();
    float* op = (float*)lds;
#pragma unroll
    for (int cb = 0; cb < 2; ++cb) {
        float* dst = op + (size_t)(kh * 256 + rg * 64 + cb * 32 + r) * 28 + 4 * h;
#pragma unroll
        for (int g = 0; g < 3; ++g) { f32x4 v = {o[cb][4 * g], o[cb][4 * g + 1], o[cb][4 * g + 2], o[cb][4 * g + 3]}; *(f32x4*)(dst + 8 * g) = v; }
    }
    int* badf = (int*)(lds + 65536 + 8 * 1296);
    if (tid == 0) *badf = 0;
    __syncthreads();
    char* ot = lds + 65536;
    int bad = 0;
    {
        const int row = tid >> 1, hf = tid & 1;
        const float* p0 = op + row * 28; const float* p1 = op + (256 + row) * 28;
        const float l = p0[20] + p1[20];
        const float inv = 1.0f / l;
        bad = !(l >= 8.271806e-25f && l < 3.0e38f);
        float vals[10];
#pragma unroll
        for (int j = 0; j < 10; ++j) { vals[j] = (p0[hf * 10 + j] + p1[hf * 10 + j]); bad |= !(fabsf(vals[j]) < 3.0e38f); vals[j] *= inv; }
        unsigned* dst = (unsigned*)(ot + (row >> 5) * 1296 + ((row & 31) * 20 + hf * 10) * 2);
#pragma unroll
        for (int j = 0; j < 5; ++j) dst[j] = pk2(vals[2 * j], vals[2 * j + 1]);
    }
    if (bad) *badf = 1;
    __syncthreads();
    if (__builtin_amdgcn_readfirstlane(*badf)) {
        const int row = c * 256 + wid * 32 + r;
        const u32x4 f1 = *(const u32x4*)(qp + (size_t)row * 20 + 8 * h);
        const u32x2 f2 = *(const u32x2*)(qp + (size_t)row * 20 + 16);
        u32x4 g2; g2[0] = h ? 0u : f2[0]; g2[1] = h ? 0u : f2[1]; g2[2] = 0u; g2[3] = 0u;
        const bf16x8 fq1 = __builtin_bit_cast(bf16x8, f1), fq2 = __builtin_bit_cast(bf16x8, g2);
        f32x16 oo = {};
        float mrun = -3.0e38f;
        const char* kv = a.ws + WS_KV;
#pragma unroll 1
        for (int T = 0; T < 64; ++T) {
            const char* b = kv + (T >> 2) * KV_STAGE + (T & 3) * KV_TILE;
            const bf16x8 k1 = *(const bf16x8*)(b + h * 512 + r * 16), k2 = *(const bf16x8*)(b + 1024 + r * 16);
            const bf16x8 vf0 = *(const bf16x8*)(b + 1536 + (h * 21 + dcl) * 16), vf1 = *(const bf16x8*)(b + 1536 + ((2 + h) * 21 + dcl) * 16);
            f32x16 s = MFMA32(k1, fq1, zero);
            s = MFMA32(k2, fq2, s);
            float mx = s[0];
#pragma unroll
            for (int j = 1; j < 16; ++j) mx = fmaxf(mx, s[j]);
            mx = fmaxf(mx, __shfl_xor(mx, 32));
            const float mn = fmaxf(mrun, mx);
            const float al = __builtin_amdgcn_exp2f(mrun - mn);
            mrun = mn;
#pragma unroll
            for (int j = 0; j < 16; ++j) oo[j] *= al;
            u32x4 p0, p1;
#pragma unroll
            for (int j = 0; j < 4; ++j) {
                p0[j] = pk2(__builtin_amdgcn_exp2f(s[2 * j] - mn), __builtin_amdgcn_exp2f(s[2 * j + 1] - mn));
                p1[j] = pk2(__builtin_amdgcn_exp2f(s[8 + 2 * j] - mn), __builtin_amdgcn_exp2f(s[8 + 2 * j + 1] - mn));
            }
            oo = MFMA32(vf0, __builtin_bit_cast(bf16x8, p0), oo);
            oo = MFMA32(vf1, __builtin_bit_cast(bf16x8, p1), oo);
        }
        const float l = __shfl(oo[8], r + 32);
        const float inv = 1.0f / l;
        const int rr = wid * 32 + r;
        unsigned* dst = (unsigned*)(ot + (rr >> 5) * 1296 + (rr & 31) * 40);
#pragma unroll
        for (int g = 0; g < 3; ++g) {
            const int d0 = 8 * g + 4 * h;
            if (d0 < 20) { dst[d0 / 2] = pk2(oo[4 * g] * inv, oo[4 * g + 1] * inv); dst[d0 / 2 + 1] = pk2(oo[4 * g + 2] * inv, oo[4 * g + 3] * inv); }
        }
        __syncthreads();
    }
    {
        char* of = a.ws + WS_OF;
        const int tb = c >> 1;
#pragma unroll
        for (int p = 0; p < 2; ++p) {
            const int i = tid + p * 512;
            if (i < 640) {
                const int sq = i & 7, kc = i >> 3;
                const u32x4 v = *(const u32x4*)(ot + sq * 1296 + kc * 16);
                *(u32x4*)(of + ((size_t)(tb * 20 + (kc >> 2)) * 64 + 8 * (c & 1) + sq + 16 * (kc & 3)) * 16) = v;
            }
        }
    }
}

constexpr int K3_LDS = 102400 + 64 * 176;
struct K3Args {
    char* ws;
    u16* out;
};
__global__ __launch_bounds__(256) void k3_out(K3Args a) {
    extern __shared__ __attribute__((aligned(16))) char lds[];
    const int tid = threadIdx.x, lane = tid & 63, wid = __builtin_amdgcn_readfirstlane(tid >> 6);
    const unsigned lds0 = (unsigned)(uintptr_t)(LDSP char*)lds;
    const int xcd = blockIdx.x & 7, idx = blockIdx.x >> 3;
    const int mt = xcd * 4 + (idx >> 3), nt = idx & 7;
    const __amdgpu_buffer_rsrc_t rW = mk_rsrc(a.ws + WS_WF, D * D * 2);
#pragma unroll
    for (int p = 0; p < 25; ++p) dma16(rW, lane * 16, nt * 102400 + (wid * 25 + p) * 1024, lds0 + (wid * 25 + p) * 1024);
    const int tb = mt * 4 + wid;
    bf16x8 of[20];
    const char* ofp = a.ws + WS_OF + (size_t)tb * 20 * 1024 + lane * 16;
#pragma unroll
    for (int s = 0; s < 20; ++s) of[s] = *(const bf16x8*)(ofp + s * 1024);
    u32x2 yp[4][5];
#pragma unroll
    for (int p = 0; p < 4; ++p)
#pragma unroll
        for (int b = 0; b < 5; ++b) yp[p][b] = *(const u32x2*)(a.ws + WS_YP + (size_t)p * S * D * 2 + ((size_t)(tb * 40 + nt * 5 + b) * 64 + lane) * 8);
    const float g = ((const float*)(a.ws + WS_G))[tb * 16 + (lane & 15)];
    WAIT_VM(0);
    BARRIER();
    f32x4 acc[5];
#pragma unroll
    for (int b = 0; b < 5; ++b) acc[b] = f32x4{0.f, 0.f, 0.f, 0.f};
#pragma unroll
    for (int s = 0; s < 20; ++s)
#pragma unroll
        for (int b = 0; b < 5; ++b) acc[b] = MFMA16(lds_ld8(lds0 + (b * 20 + s) * 1024 + lane * 16), of[s], acc[b]);
    char* ot = lds + 102400;
#pragma unroll
    for (int b = 0; b < 5; ++b) {
        float y[4];
#pragma unroll
        for (int j = 0; j < 4; ++j) {
            float sum = 0.f;
#pragma unroll
            for (int p = 0; p < 4; ++p) sum += (j & 1) ? __uint_as_float(yp[p][b][j >> 1] & 0xffff0000u) : __uint_as_float(yp[p][b][j >> 1] << 16);
            y[j] = g * acc[b][j] + (1.0f - g) * sum;
        }
        u32x2 v; v[0] = pk2(y[0], y[1]); v[1] = pk2(y[2], y[3]);
        *(u32x2*)(ot + (wid * 16 + (lane & 15)) * 176 + (16 * b + 4 * (lane >> 4)) * 2) = v;
    }
    __syncthreads();
#pragma unroll
    for (int p = 0; p < 3; ++p) {
        const int i = tid + p * 256;
        if (i < 640) {
            const int row = i / 10, ch = i % 10;
            const u32x4 v = *(const u32x4*)(ot + row * 176 + ch * 16);
            *(u32x4*)(a.out + (size_t)(mt * 64 + row) * D + nt * 80 + ch * 8) = v;
        }
    }
}

extern "C" void kernel_launch(void* const* d_in, const int* in_sizes, int n_in, void* d_out, int out_size, void* d_ws, size_t ws_size, hipStream_t stream) {
    (void)in_sizes; (void)n_in; (void)out_size; (void)ws_size;
    (void)hipFuncSetAttribute((const void*)k1_proj, hipFuncAttributeMaxDynamicSharedMemorySize, K1_LDS);
    (void)hipFuncSetAttribute((const void*)k2_main, hipFuncAttributeMaxDynamicSharedMemorySize, K2_LDS);
    (void)hipFuncSetAttribute((const void*)k3_out, hipFuncAttributeMaxDynamicSharedMemorySize, K3_LDS);
    K1Args a1;
    a1.xa = (const u16*)d_in[0]; a1.wq = (const u16*)d_in[1]; a1.wk = (const u16*)d_in[2]; a1.wv = (const u16*)d_in[3]; a1.wo = (const u16*)d_in[4];
    a1.xb = (const u16*)d_in[0]; a1.wr = (const u16*)d_in[8]; a1.wi = (const u16*)d_in[9]; a1.gw = (const u16*)d_in[10];
    a1.ws = (char*)d_ws;
    hipLaunchKernelGGL(k1_proj, dim3(256), dim3(256), K1_LDS, stream, a1);
    K2Args a2; a2.cre = (const u16*)d_in[6]; a2.cim = (const u16*)d_in[7]; a2.ws = (char*)d_ws;
    hipLaunchKernelGGL(k2_main, dim3(256), dim3(512), K2_LDS, stream, a2);
    K3Args a3; a3.ws = (char*)d_ws; a3.out = (u16*)d_out;
    hipLaunchKernelGGL(k3_out, dim3(256), dim3(256), K3_LDS, stream, a3);
}
```

```cpp
#include <hip/hip_runtime.h>
#include <hip/hip_bf16.h>
#include <stdint.h>

typedef __bf16 bf16x8 __attribute__((ext_vector_type(8)));
typedef __bf16 bf16x4 __attribute__((ext_vector_type(4)));
typedef __bf16 bf16x2 __attribute__((ext_vector_type(2)));
typedef short s16x4 __attribute__((ext_vector_type(4)));
typedef float f32x16 __attribute__((ext_vector_type(16)));
typedef float f32x4 __attribute__((ext_vector_type(4)));
typedef float f32x2 __attribute__((ext_vector_type(2)));
typedef unsigned u32x4 __attribute__((ext_vector_type(4)));
typedef unsigned u32x2 __attribute__((ext_vector_type(2)));
typedef unsigned short u16;
#define LDSP __attribute__((address_space(3)))
#define DI __device__ __forceinline__
#define MFMA32(a, b, c) __builtin_amdgcn_mfma_f32_32x32x16_bf16((a), (b), (c), 0, 0, 0)
#define MFMA16(a, b, c) __builtin_amdgcn_mfma_f32_16x16x32_bf16((a), (b), (c), 0, 0, 0)
#define SBAR() __builtin_amdgcn_sched_barrier(0)
#define STAMP(ws, kern, i)
#define STAMPR(ws, kern, i)

constexpr int S = 2048, D = 640, NH = 32, DH = 20;
constexpr float QSCALE = 0.22360679774997896f * 1.4426950408889634f;

constexpr size_t WS_QP = 0;
constexpr size_t WS_KV = WS_QP + (size_t)S * D * 2;
constexpr int KV_STAGE = 12288, KV_TILE = 2880;
constexpr size_t WS_BT = WS_KV + 16 * KV_STAGE;
constexpr size_t WS_WF = WS_BT + (size_t)1280 * S * 2;
constexpr size_t WS_G = WS_WF + (size_t)D * D * 2;
constexpr size_t WS_KM = WS_G + S * 4;
constexpr size_t WS_OF = WS_KM + 256;
constexpr size_t WS_YP = WS_OF + (size_t)S * D * 2;
constexpr size_t WS_END = WS_YP + (size_t)4 * S * D * 2;

DI unsigned pk2(float a, float b) { f32x2 v = {a, b}; bf16x2 r = __builtin_convertvector(v, bf16x2); return __builtin_bit_cast(unsigned, r); }
DI float bf2f(u16 x) { return __uint_as_float((unsigned)x << 16); }
DI __amdgpu_buffer_rsrc_t mk_rsrc(const void* p, unsigned bytes) { return __builtin_amdgcn_make_buffer_rsrc((void*)p, (short)0, (int)bytes, 0x00020000); }
DI void dma16(__amdgpu_buffer_rsrc_t rs, unsigned voff, unsigned soff, unsigned lds) {
    asm volatile("s_mov_b32 m0, %3\n\ts_nop 0\n\tbuffer_load_dwordx4 %0, %1, %2 offen lds" ::"v"(voff), "s"(rs), "s"(soff), "s"(lds) : "memory");
}
#define WAIT_VM(n) asm volatile("s_waitcnt vmcnt(" #n ")" ::: "memory")
#define WAIT_LGKM0() asm volatile("s_waitcnt lgkmcnt(0)" ::: "memory")
#define BARRIER() asm volatile("s_barrier" ::: "memory")
DI bf16x8 lds_ld8(unsigned a) { return *(const LDSP bf16x8*)(uintptr_t)a; }
#define LLD(dst, a) dst = lds_ld8(a)
DI s16x4 lds_tr(unsigned a) { return __builtin_amdgcn_ds_read_tr16_b64_v4i16((LDSP s16x4*)(uintptr_t)a); }

constexpr int K1_STAGE = 32768, K1_NS = 5, K1_LDS = K1_STAGE * K1_NS;
struct K1Args {
    const u16 *xa, *wq, *wk, *wv, *wo, *wr, *wi, *gw;
    char* ws;
};
template <int N> DI void wait_vm() { asm volatile("s_waitcnt vmcnt(%0)" ::"n"(N) : "memory"); }

template <int MODE>
DI void k1_gemm(f32x16 (&acc)[2], unsigned lds0, __amdgpu_buffer_rsrc_t rx, unsigned xrow0, __amdgpu_buffer_rsrc_t rw, unsigned wbase, int wid, int lane, char* ws) {
    const unsigned xr = lane >> 3, xc = lane & 7;
    const unsigned xv0 = (16 * wid + xr) * 1280 + ((xc ^ (xr >> 1)) << 4);
    const unsigned xv1 = (16 * wid + 8 + xr) * 1280 + ((xc ^ (4 + (xr >> 1))) << 4);
    unsigned wv0, wv1;
    if (MODE == 0) {
        const unsigned kr = lane >> 4, cc = lane & 15;
        wv0 = (8 * wid + kr) * 1280 + (((((cc >> 2) ^ kr) << 2) | (cc & 3)) << 4);
        wv1 = wv0 + 4 * 1280;
    } else {
        wv0 = xv0; wv1 = xv1;
    }
    auto issue2 = [&](int st, int slot_) {
        const unsigned sl = lds0 + slot_ * K1_STAGE + wid * 2048;
        const unsigned xs = xrow0 * 1280 + st * 128;
        const unsigned wsb = wbase + (MODE == 0 ? st * 64 * 1280 : st * 128);
        dma16(rx, xv0, xs, sl); dma16(rw, wv0, wsb, sl + 16384);
        dma16(rx, xv1, xs, sl + 1024); dma16(rw, wv1, wsb, sl + 16384 + 1024);
    };
    issue2(0, 0); issue2(1, 1); issue2(2, 2); issue2(3, 3);
    STAMP(ws, 0, 1);
    const int r = lane & 31, h = lane >> 5;
    const int wi = wid >> 1, wj = wid & 1;
    const unsigned sw = (r >> 1) & 7;
    const unsigned tq = (lane & 15) >> 2;
    unsigned xo[4], wo_;
#pragma unroll
    for (int ks = 0; ks < 4; ++ks) xo[ks] = r * 128 + (((2 * ks + h) ^ sw) << 4);
    const unsigned xblk = (MODE == 0) ? wj * 64 * 128 : wi * 32 * 128;
    if (MODE == 0) wo_ = 16384 + (8 * h + tq) * 256 + ((lane >> 4) & 1) * 32 + (lane & 3) * 8 + ((wi ^ tq) << 6);
    else wo_ = 16384 + wj * 64 * 128;
#pragma unroll
    for (int st = 0; st < 10; ++st) {
        if (st <= 6) wait_vm<12>(); else if (st == 7) wait_vm<8>(); else if (st == 8) wait_vm<4>(); else wait_vm<0>();
        BARRIER();
        if (st == 0) STAMP(ws, 0, 2);
        if (st + 4 < 10) issue2(st + 4, (st + 4) % K1_NS);
        const unsigned sl = lds0 + (st % K1_NS) * K1_STAGE;
        bf16x8 f0[4], f1[4], f2[4];
#pragma unroll
        for (int ks = 0; ks < 4; ++ks) {
            if (MODE == 0) {
                const s16x4 lo = lds_tr(sl + wo_ + ks * 4096), hi = lds_tr(sl + wo_ + ks * 4096 + 1024);
                f0[ks] = __builtin_bit_cast(bf16x8, __builtin_shufflevector(lo, hi, 0, 1, 2, 3, 4, 5, 6, 7));
                f1[ks] = lds_ld8(sl + xblk + xo[ks]); f2[ks] = lds_ld8(sl + xblk + 4096 + xo[ks]);
            } else {
                f0[ks] = lds_ld8(sl + xblk + xo[ks]);
                f1[ks] = lds_ld8(sl + wo_ + xo[ks]); f2[ks] = lds_ld8(sl + wo_ + 4096 + xo[ks]);
            }
        }
        SBAR();
#pragma unroll
        for (int ks = 0; ks < 4; ++ks) { acc[0] = MFMA32(f0[ks], f1[ks], acc[0]); acc[1] = MFMA32(f0[ks], f2[ks], acc[1]); }
        SBAR();
        WAIT_LGKM0();
    }
    BARRIER();
}

DI void k1_store_tile(const f32x16 (&acc)[2], float scale, char* lds, u16* dst, int pitch_elems, int tid, int wid, int lane) {
    const int r = lane & 31, h = lane >> 5, wi = wid >> 1, wj = wid & 1;
#pragma unroll
    for (int bj = 0; bj < 2; ++bj)
#pragma unroll
        for (int g = 0; g < 4; ++g) {
            u32x2 v; v[0] = pk2(acc[bj][4 * g] * scale, acc[bj][4 * g + 1] * scale); v[1] = pk2(acc[bj][4 * g + 2] * scale, acc[bj][4 * g + 3] * scale);
            const int row = wj * 64 + bj * 32 + r, col = wi * 32 + 8 * g + 4 * h;
            *(u32x2*)(lds + row * 272 + col * 2) = v;
        }
    __syncthreads();
#pragma unroll
    for (int p = 0; p < 4; ++p) {
        const int row = p * 32 + (tid >> 4), ch = tid & 15;
        const u32x4 v = *(const u32x4*)(lds + row * 272 + ch * 16);
        *(u32x4*)(dst + (size_t)row * pitch_elems + ch * 8) = v;
    }
}

__global__ __launch_bounds__(512) void k1_proj(K1Args a) {
    extern __shared__ __attribute__((aligned(16))) char lds[];
    const int tid = threadIdx.x, lane = tid & 63, wid = __builtin_amdgcn_readfirstlane(tid >> 6);
    const int xcd = blockIdx.x & 7, idx = blockIdx.x >> 3;
    const int mb = 4 * (xcd >> 1) + (idx >> 3), t = 8 * (xcd & 1) + (idx & 7);
    STAMP(a.ws, 0, 0); STAMPR(a.ws, 0, 6);
    const unsigned lds0 = (unsigned)(uintptr_t)(LDSP char*)lds;
    f32x16 acc[2];
#pragma unroll
    for (int k = 0; k < 16; ++k) { acc[0][k] = 0.f; acc[1][k] = 0.f; }
    if (t < 5) {
        k1_gemm<0>(acc, lds0, mk_rsrc(a.xa, S * D * 2), mb * 128, mk_rsrc(a.wq, D * D * 2), t * 256, wid, lane, a.ws);
        STAMP(a.ws, 0, 3);
        k1_store_tile(acc, QSCALE, lds, (u16*)(a.ws + WS_QP) + (size_t)mb * 128 * D + t * 128, D, tid, wid, lane);
    } else if (t >= 6) {
        const int eb = t - 6;
        const int pair = mb * 10 + eb, nt = pair / 20, step = pair % 20;
        u16 wv[8];
        {
            const int ch = tid < 320 ? tid : 0;
            const int e = 80 * nt + 16 * (ch >> 6) + (ch & 15), k0 = 32 * step + 8 * ((ch & 63) >> 4);
#pragma unroll
            for (int j = 0; j < 8; ++j) wv[j] = a.wo[(size_t)(k0 + j) * D + e];
        }
        const u16* w = eb < 5 ? a.wr : a.wi;
        k1_gemm<1>(acc, lds0, mk_rsrc(a.xa, S * D * 2), mb * 128, mk_rsrc(w, D * D * 2), (eb < 5 ? eb : eb - 5) * 128 * 1280, wid, lane, a.ws);
        STAMP(a.ws, 0, 3);
        if (tid < 320) {
            u32x4 v;
#pragma unroll
            for (int j = 0; j < 4; ++j) v[j] = (unsigned)wv[2 * j] | ((unsigned)wv[2 * j + 1] << 16);
            *(u32x4*)(a.ws + WS_WF + ((size_t)(nt * 5 + (tid >> 6)) * 20 + step) * 1024 + (tid & 63) * 16) = v;
        }
        k1_store_tile(acc, eb < 5 ? 1.f : -1.f, lds, (u16*)(a.ws + WS_BT) + (size_t)eb * 128 * S + mb * 128, S, tid, wid, lane);
    } else {
        const __amdgpu_buffer_rsrc_t rx = mk_rsrc(a.xa, S * D * 2);
        const unsigned xr = lane >> 3, xc = lane & 7;
        const unsigned xv0 = (16 * wid + xr) * 1280 + ((xc ^ (xr >> 1)) << 4);
        const unsigned xv1 = (16 * wid + 8 + xr) * 1280 + ((xc ^ (4 + (xr >> 1))) << 4);
        const unsigned xl0 = lds0 + 81920;
        u32x2 wl[2][10]; u16 gl[2];
#pragma unroll
        for (int p = 0; p < 2; ++p) {
            const int k = (tid + p * 512) < 640 ? tid + p * 512 : 0;
#pragma unroll
            for (int c = 0; c < 5; ++c) { wl[p][c] = *(const u32x2*)(a.wk + k * 20 + c * 4); wl[p][5 + c] = *(const u32x2*)(a.wv + k * 20 + c * 4); }
            gl[p] = a.gw[k];
        }
        auto issue = [&](int st) {
            const unsigned sl = xl0 + (st & 3) * 16384 + wid * 2048;
            dma16(rx, xv0, mb * 128 * 1280 + st * 128, sl); dma16(rx, xv1, mb * 128 * 1280 + st * 128, sl + 1024);
        };
        issue(0); issue(1); issue(2);
#pragma unroll
        for (int p = 0; p < 2; ++p) {
            const int k = tid + p * 512;
            if (k < 640) {
                char* row = lds + k * 128;
                const int sx = ((k >> 1) & 1) << 6;
#pragma unroll
                for (int c = 0; c < 10; ++c) *(u32x2*)(row + ((c * 8) ^ sx)) = wl[p][c];
                *(u16*)(row + (80 ^ sx)) = gl[p];
            }
        }
        const int r = lane & 31, h = lane >> 5;
        const int sb = wid >> 1, nb = wid & 1;
        const unsigned sw = (r >> 1) & 7, tq = (lane & 15) >> 2;
        const unsigned wofs = (8 * h + tq) * 128 + ((nb ^ ((tq >> 1) & 1)) << 6) + ((lane >> 4) & 1) * 32 + (lane & 3) * 8;
        f32x16 c0 = acc[0];
#pragma unroll
        for (int st = 0; st < 10; ++st) {
            if (st <= 7) wait_vm<4>(); else if (st == 8) wait_vm<2>(); else wait_vm<0>();
            WAIT_LGKM0();
            BARRIER();
            if (st + 3 < 10) issue(st + 3);
            const unsigned sl = xl0 + (st & 3) * 16384;
            bf16x8 xf[4], wf[4];
#pragma unroll
            for (int ks = 0; ks < 4; ++ks) {
                xf[ks] = lds_ld8(sl + sb * 4096 + r * 128 + (((2 * ks + h) ^ sw) << 4));
                const unsigned wa = lds0 + (st * 64 + ks * 16) * 128 + wofs;
                const s16x4 lo = lds_tr(wa), hi = lds_tr(wa + 512);
                wf[ks] = __builtin_bit_cast(bf16x8, __builtin_shufflevector(lo, hi, 0, 1, 2, 3, 4, 5, 6, 7));
            }
            SBAR();
#pragma unroll
            for (int ks = 0; ks < 4; ++ks) c0 = MFMA32(wf[ks], xf[ks], c0);
            SBAR();
        }
        WAIT_LGKM0();
        BARRIER();
        float* T = (float*)lds;
#pragma unroll
        for (int g = 0; g < 4; ++g) {
            f32x4 v0 = {c0[4 * g], c0[4 * g + 1], c0[4 * g + 2], c0[4 * g + 3]};
            *(f32x4*)(T + (sb * 32 + r) * 68 + nb * 32 + 8 * g + 4 * h) = v0;
        }
        __syncthreads();
        char* img = a.ws + WS_KV + (size_t)mb * KV_STAGE;
        if (tid < 384) {
            const int i = tid, tt = i / 96, c = (i % 96) >> 5, key = i & 31;
            const float* row = T + (tt * 32 + key) * 68;
            u32x4 v;
            if (c < 2) { v[0] = pk2(row[8 * c], row[8 * c + 1]); v[1] = pk2(row[8 * c + 2], row[8 * c + 3]); v[2] = pk2(row[8 * c + 4], row[8 * c + 5]); v[3] = pk2(row[8 * c + 6], row[8 * c + 7]); }
            else { v[0] = pk2(row[16], row[17]); v[1] = pk2(row[18], row[19]); v[2] = 0x00003F80u; v[3] = 0u; }
            *(u32x4*)(img + tt * KV_TILE + c * 512 + key * 16) = v;
        }
        if (tid < 4 * 84) {
            const int i = tid, tt = i / 84, q = i % 84, sh = q / 21, d = q % 21, s2 = sh >> 1, hh = sh & 1;
            float e[8];
#pragma unroll
            for (int j = 0; j < 8; ++j) { const int key = 16 * s2 + 8 * (j >> 2) + 4 * hh + (j & 3); e[j] = d < 20 ? T[(tt * 32 + key) * 68 + 20 + d] : 1.0f; }
            u32x4 v; v[0] = pk2(e[0], e[1]); v[1] = pk2(e[2], e[3]); v[2] = pk2(e[4], e[5]); v[3] = pk2(e[6], e[7]);
            *(u32x4*)(img + tt * KV_TILE + 1536 + q * 16) = v;
        }
        float n2 = 0.f;
        if (tid >= 384) {
            const int rr = tid - 384;
            const float* row = T + rr * 68;
#pragma unroll
            for (int d = 0; d < 20; ++d) { const float kv = bf2f((u16)(pk2(row[d], 0.f) & 0xffff)); n2 += kv * kv; }
            ((float*)(a.ws + WS_G))[mb * 128 + rr] = 1.0f / (1.0f + __expf(-row[40]));
#pragma unroll
            for (int o = 32; o >= 1; o >>= 1) n2 = fmaxf(n2, __shfl_xor(n2, o));
            if (lane == 0) ((float*)(a.ws + WS_KM))[mb * 2 + (wid & 1)] = n2;
        }
    }
    STAMP(a.ws, 0, 4); STAMPR(a.ws, 0, 7);
}

constexpr int K2_A = 0, K2_B = 3 * 16384, K2_KV = K2_B + 3 * 20480, K2_Q = K2_KV + 3 * KV_STAGE, K2_LDS = K2_Q + 10240;
struct K2Args {
    const u16 *cre, *cim;
    char* ws;
};

#define PIN(x) asm volatile("" : "+v"(x))
#define EXP2(x) __builtin_amdgcn_exp2f(x)
struct K2Regs {
    f32x16 s[2];
    u32x4 p0, p1;
    bf16x8 kA1, kA2, kB1, kB2, vA0, vA1, vB0, vB1;
    bf16x8 af[2], bf[2][3];
};
#define EX(i) sc[i] = EXP2(sc[i])
#define CV0(i) p0[i] = pk2(sc[2 * (i)], sc[2 * (i) + 1])
#define CV1(i) p1[i] = pk2(sc[8 + 2 * (i)], sc[8 + 2 * (i) + 1])
#define GE() do { PIN(sc); SBAR(); } while (0)
#define GE0() do { PIN(sc); PIN(p0); SBAR(); } while (0)
#define GE1() do { PIN(sc); PIN(p1); SBAR(); } while (0)
struct K2Dma {
    __amdgpu_buffer_rsrc_t rA, rB, rK;
    unsigned v0, v1, vk;
    unsigned sA, sB, sK;
    unsigned lA, lB, lK;
    unsigned bpar;
};
template <int NB, int I>
DI void k2_dma(const K2Dma& d, int st, int sl) {
    if (st < 16) {
        constexpr int NKV = NB == 3 ? 1 : 2;
        if (I < NKV) dma16(d.rK, d.vk, d.sK + st * KV_STAGE + I * 1024, d.lK + sl * KV_STAGE + I * 1024);
        else if (I < NKV + 2) dma16(d.rA, (I - NKV) ? d.v1 : d.v0, d.sA + st * 128 + (I - NKV) * 8 * 4096, d.lA + sl * 16384 + (I - NKV) * 1024);
        else { constexpr int P = I - NKV - 2; dma16(d.rB, (NB == 3 ? ((P & 1) ^ d.bpar) : (P & 1)) ? d.v1 : d.v0, d.sB + st * 128 + P * 8 * 4096, d.lB + sl * 20480 + P * 1024); }
    }
}
template <int NB> DI void k2_dma_kv(const K2Dma& d, int st, int sl) { k2_dma<NB, 0>(d, st, sl); if (NB == 2) k2_dma<NB, 1>(d, st, sl); }
template <int NB> DI void k2_dma_ab(const K2Dma& d, int st, int sl) {
    if (NB == 3) k2_dma<NB, 1>(d, st, sl);
    k2_dma<NB, 2>(d, st, sl); k2_dma<NB, 3>(d, st, sl); k2_dma<NB, 4>(d, st, sl); k2_dma<NB, 5>(d, st, sl);
}
struct K2Addr { unsigned ga[4], gb[4], kb1, kb2, vb; };

template <int NB, int SL, int U>
DI void k2_unit(K2Regs& R, f32x16 (&o)[2], f32x16 (&acc)[3], const bf16x8 (&q1)[2], const bf16x8 (&q2)[2], const K2Addr& A, const K2Dma& d, int st) {
    constexpr int SN = (SL + 1) % 3, S2 = (SL + 2) % 3;
    f32x16& sc = R.s[U & 1]; f32x16& sn = R.s[(U + 1) & 1];
    u32x4 &p0 = R.p0, &p1 = R.p1;
    f32x16& ocur = o[U & 1]; f32x16& oprev = o[(U + 1) & 1];
    const bf16x8& kn1 = (U == 0 || U == 3) ? R.kA1 : R.kB1; const bf16x8& kn2 = (U == 0 || U == 3) ? R.kA2 : R.kB2;
    const bf16x8& qn1 = q1[(U + 1) & 1]; const bf16x8& qn2 = q2[(U + 1) & 1];
    const bf16x8& vprev1 = (U == 0 || U == 3) ? R.vB1 : R.vA1;
    const bf16x8& vcur0 = (U < 2) ? R.vA0 : R.vB0;
    const bf16x8& af = R.af[(U + 1) & 1]; const bf16x8 (&bf)[3] = R.bf[(U + 1) & 1];
    bf16x8& afn = R.af[U & 1]; bf16x8 (&bfn)[3] = R.bf[U & 1];
    const f32x16 zero = {};
    auto ld_a = [&]() {
        if (U == 0) LLD(R.kB1, A.kb1 + SL * KV_STAGE + KV_TILE);
        if (U == 1) LLD(R.vB0, A.vb + SL * KV_STAGE + KV_TILE);
        if (U == 2) LLD(R.kA1, A.kb1 + SN * KV_STAGE);
        if (U == 3) LLD(R.vA0, A.vb + SN * KV_STAGE);
        SBAR();
    };
    auto ld_b = [&]() {
        if (U == 0) LLD(R.kB2, A.kb2 + SL * KV_STAGE + KV_TILE);
        if (U == 1) LLD(R.vB1, A.vb + SL * KV_STAGE + KV_TILE + 672);
        if (U == 2) LLD(R.kA2, A.kb2 + SN * KV_STAGE);
        if (U == 3) LLD(R.vA1, A.vb + SN * KV_STAGE + 672);
        SBAR();
    };
    auto ld_g = [&](int i) {
        if (i == 0) LLD(afn, A.ga[U] + SL * 16384); else LLD(bfn[i - 1], A.gb[U] + SL * 20480 + (i - 1) * 4096);
        SBAR();
    };
    auto dm = [&](int k) {
        if (U < 3) { if (k == 0) k2_dma<NB, 2 * (U < 3 ? U : 0)>(d, st + 2, S2); else k2_dma<NB, 2 * (U < 3 ? U : 0) + 1>(d, st + 2, S2); SBAR(); }
    };
    __builtin_amdgcn_s_setprio(3 - U);
    sn = MFMA32(kn1, qn1, zero); PIN(sn); SBAR();
    EX(0); SBAR(); EX(1); SBAR(); EX(2); GE();
    oprev = MFMA32(vprev1, __builtin_bit_cast(bf16x8, p1), oprev); PIN(oprev); SBAR();
    if (NB == 2) { EX(3); SBAR(); EX(4); SBAR(); EX(5); SBAR(); CV0(0); GE0(); } else { EX(3); SBAR(); EX(4); SBAR(); CV0(0); SBAR(); CV0(1); GE0(); }
    sn = MFMA32(kn2, qn2, sn); PIN(sn); SBAR();
    ld_a(); ld_b();
    if (NB == 2) { EX(6); SBAR(); EX(7); SBAR(); CV0(1); SBAR(); CV0(2); SBAR(); CV0(3); GE0(); } else { EX(5); SBAR(); EX(6); SBAR(); EX(7); GE(); }
    dm(0);
    acc[0] = MFMA32(bf[0], af, acc[0]); PIN(acc[0]); SBAR();
    ld_g(0); ld_g(1);
    if (NB == 2) { EX(8); SBAR(); EX(9); SBAR(); EX(10); SBAR(); CV1(0); GE1(); } else { EX(8); SBAR(); EX(9); SBAR(); CV0(2); SBAR(); CV0(3); GE0(); EX(10); SBAR(); }
    ocur = MFMA32(vcur0, __builtin_bit_cast(bf16x8, p0), ocur); PIN(ocur); SBAR();
    ld_g(2);
    if (NB == 2) { EX(11); SBAR(); EX(12); SBAR(); EX(13); SBAR(); CV1(1); GE1(); } else { EX(11); SBAR(); CV1(0); SBAR(); CV1(1); GE1(); }
    dm(1);
    acc[1] = MFMA32(bf[1], af, acc[1]); PIN(acc[1]); SBAR();
    if (NB == 2) { EX(14); SBAR(); EX(15); SBAR(); CV1(2); SBAR(); CV1(3); GE1(); }
    else {
        EX(12); SBAR(); EX(13); SBAR(); EX(14); GE();
        acc[2] = MFMA32(bf[2], af, acc[2]); PIN(acc[2]); SBAR();
        ld_g(3);
        EX(15); SBAR(); CV1(2); SBAR(); CV1(3); GE1();
    }
}
template <int NB, int SL>
DI void k2_stage(int st, K2Regs& R, f32x16 (&o)[2], f32x16 (&acc)[3], const bf16x8 (&q1)[2], const bf16x8 (&q2)[2], const K2Addr& A, const K2Dma& d) {
    if (st <= 14) { if (NB == 3) WAIT_VM(5); else WAIT_VM(4); } else { WAIT_VM(0); }
    BARRIER();
    SBAR();
    k2_unit<NB, SL, 0>(R, o, acc, q1, q2, A, d, st);
    k2_unit<NB, SL, 1>(R, o, acc, q1, q2, A, d, st);
    k2_unit<NB, SL, 2>(R, o, acc, q1, q2, A, d, st);
    k2_unit<NB, SL, 3>(R, o, acc, q1, q2, A, d, st);
}
template <int NB>
DI void k2_loop(f32x16 (&o)[2], f32x16 (&acc)[3], const bf16x8 (&q1)[2], const bf16x8 (&q2)[2], const K2Addr& A, const K2Dma& d, char* ws) {
    K2Regs R;
    const f32x16 zero = {};
    const u32x4 z4 = {0u, 0u, 0u, 0u};
    R.kA1 = lds_ld8(A.kb1); R.kA2 = lds_ld8(A.kb2); R.vA0 = lds_ld8(A.vb); R.vA1 = lds_ld8(A.vb + 672);
    R.af[1] = __builtin_bit_cast(bf16x8, z4);
#pragma unroll
    for (int b = 0; b < 3; ++b) { R.bf[1][b] = R.af[1]; R.bf[0][b] = R.af[1]; }
    R.af[0] = R.af[1];
    R.s[0] = MFMA32(R.kA1, q1[0], zero); R.s[0] = MFMA32(R.kA2, q2[0], R.s[0]);
    R.p1 = z4; R.p0 = z4; R.vB1 = R.vA1; R.vB0 = R.vA0; R.kB1 = R.kA1; R.kB2 = R.kA2;
    STAMP(ws, 1, 1);
#pragma unroll 1
    for (int st = 0; st < 15; st += 3) {
        k2_stage<NB, 0>(st, R, o, acc, q1, q2, A, d);
        k2_stage<NB, 1>(st + 1, R, o, acc, q1, q2, A, d);
        k2_stage<NB, 2>(st + 2, R, o, acc, q1, q2, A, d);
    }
    k2_stage<NB, 0>(15, R, o, acc, q1, q2, A, d);
    __builtin_amdgcn_s_setprio(0);
    o[1] = MFMA32(R.vB1, __builtin_bit_cast(bf16x8, R.p1), o[1]);
#pragma unroll
    for (int b = 0; b < NB; ++b) acc[b] = MFMA32(R.bf[1][b], R.af[1], acc[b]);
}

__global__ __launch_bounds__(512) void k2_main(K2Args a) {
    extern __shared__ __attribute__((aligned(16))) char lds[];
    const int tid = threadIdx.x, lane = tid & 63, wid = __builtin_amdgcn_readfirstlane(tid >> 6);
    const int r = lane & 31, h = lane >> 5;
    const unsigned lds0 = (unsigned)(uintptr_t)(LDSP char*)lds;
    const int c = blockIdx.x, xcd = c & 7, idx = c >> 3;
    STAMP(a.ws, 1, 0); STAMPR(a.ws, 1, 6);
    const int kq = xcd >> 1, mt = (xcd & 1) * 8 + (idx >> 2), nt = idx & 3;
    const int mi = wid & 3, nj = wid >> 2;
    const int rg = wid >> 1, kh = wid & 1;
    K2Dma d;
    d.rA = mk_rsrc((kq >> 1) ? a.cim : a.cre, S * S * 2);
    d.rB = mk_rsrc(a.ws + WS_BT, 1280 * S * 2);
    d.rK = mk_rsrc(a.ws + WS_KV, 16 * KV_STAGE);
    {
        const unsigned xr = lane >> 3, xc = lane & 7;
        d.v0 = xr * 4096 + ((xc ^ ((xr >> 1) & 7)) << 4); d.v1 = xr * 4096 + ((xc ^ ((4 + (xr >> 1)) & 7)) << 4); d.vk = lane * 16;
        const unsigned abase = (unsigned)(mt * 128) * 4096 + (kq & 1) * 2048;
        const unsigned bbase = (unsigned)((kq >> 1) * 640 + nt * 160) * 4096 + (kq & 1) * 2048;
        const int pb = wid < 4 ? 3 * wid : 12 + 2 * (wid - 4), pk = wid < 4 ? wid : 4 + 2 * (wid - 4);
        d.sA = abase + (2 * wid) * 8 * 4096; d.lA = lds0 + K2_A + 2 * wid * 1024;
        d.sB = bbase + pb * 8 * 4096; d.lB = lds0 + K2_B + pb * 1024; d.bpar = pb & 1;
        d.sK = pk * 1024; d.lK = lds0 + K2_KV + pk * 1024;
    }
    {
        const __amdgpu_buffer_rsrc_t rQ = mk_rsrc(a.ws + WS_QP, S * D * 2);
        dma16(rQ, d.vk, c * 10240 + wid * 1024, lds0 + K2_Q + wid * 1024);
        if (wid < 2) dma16(rQ, d.vk, c * 10240 + (8 + wid) * 1024, lds0 + K2_Q + (8 + wid) * 1024);
    }
    if (wid < 4) { k2_dma_kv<3>(d, 0, 0); k2_dma_kv<3>(d, 1, 1); k2_dma_ab<3>(d, 0, 0); } else { k2_dma_kv<2>(d, 0, 0); k2_dma_kv<2>(d, 1, 1); k2_dma_ab<2>(d, 0, 0); }
    const float* kmp = (const float*)(a.ws + WS_KM);
    float km = kmp[0];
#pragma unroll
    for (int i = 1; i < 32; ++i) km = fmaxf(km, kmp[i]);
    WAIT_VM(6);
    BARRIER();
    bf16x8 q1[2], q2[2];
#pragma unroll
    for (int cb = 0; cb < 2; ++cb) {
        const char* qrow = lds + K2_Q + (rg * 64 + cb * 32 + r) * 40;
        const u32x2 fa = *(const u32x2*)(qrow + 16 * h), fb = *(const u32x2*)(qrow + 16 * h + 8);
        const u32x2 f2 = *(const u32x2*)(qrow + 32);
        u32x4 f1; f1[0] = fa[0]; f1[1] = fa[1]; f1[2] = fb[0]; f1[3] = fb[1];
        float n2 = 0.f;
#pragma unroll
        for (int j = 0; j < 4; ++j) { const float x = __uint_as_float(f1[j] << 16), y = __uint_as_float(f1[j] & 0xffff0000u); n2 += x * x + y * y; }
        if (h == 0) {
#pragma unroll
            for (int j = 0; j < 2; ++j) { const float x = __uint_as_float(f2[j] << 16), y = __uint_as_float(f2[j] & 0xffff0000u); n2 += x * x + y * y; }
        }
        { auto rr = __builtin_amdgcn_permlane32_swap(__float_as_uint(n2), __float_as_uint(n2), false, false); n2 = __uint_as_float(rr[0]) + __uint_as_float(rr[1]); }
        const float m = __builtin_sqrtf(n2 * km) * 1.01f - 64.f;
        u32x4 g2; g2[0] = h ? 0u : f2[0]; g2[1] = h ? 0u : f2[1]; g2[2] = h ? 0u : (pk2(-m, 0.f) & 0xffffu); g2[3] = 0u;
        q1[cb] = __builtin_bit_cast(bf16x8, f1); q2[cb] = __builtin_bit_cast(bf16x8, g2);
    }
    f32x16 o[2], acc[3];
#pragma unroll
    for (int k = 0; k < 16; ++k) { o[0][k] = 0.f; o[1][k] = 0.f; acc[0][k] = 0.f; acc[1][k] = 0.f; acc[2][k] = 0.f; }
    const unsigned sw = (r >> 1) & 7;
    const int dcl = r <= 20 ? r : (r < 28 ? 20 : r - 12);
    const f32x16 zero = {};
    {
        if (wid < 4) k2_dma_ab<3>(d, 1, 1); else k2_dma_ab<2>(d, 1, 1);
        K2Addr A;
#pragma unroll
        for (int ks = 0; ks < 4; ++ks) {
            A.ga[ks] = lds0 + K2_A + mi * 4096 + r * 128 + (((2 * ks + h) ^ sw) << 4);
            A.gb[ks] = lds0 + K2_B + nj * 3 * 4096 + r * 128 + (((2 * ks + h) ^ sw) << 4);
            PIN(A.ga[ks]); PIN(A.gb[ks]);
        }
        A.kb1 = lds0 + K2_KV + kh * 2 * KV_TILE + h * 512 + r * 16;
        A.kb2 = lds0 + K2_KV + kh * 2 * KV_TILE + 1024 + r * 16;
        A.vb = lds0 + K2_KV + kh * 2 * KV_TILE + 1536 + (h * 21 + dcl) * 16;
        PIN(A.kb1); PIN(A.kb2); PIN(A.vb);
        if (nj == 0) k2_loop<3>(o, acc, q1, q2, A, d, a.ws);
        else k2_loop<2>(o, acc, q1, q2, A, d, a.ws);
    }
    STAMP(a.ws, 1, 2);
    {
        char* yp = a.ws + WS_YP + (size_t)kq * S * D * 2;
        const int t = mt * 128 + mi * 32 + r, tb = t >> 4;
        const int nb = nj == 0 ? 3 : 2;
#pragma unroll
        for (int b = 0; b < 3; ++b) {
            if (b < nb) {
                const int e0 = nt * 160 + (nj * 3 + b) * 32;
#pragma unroll
                for (int g = 0; g < 4; ++g) {
                    u32x2 v; v[0] = pk2(acc[b][4 * g], acc[b][4 * g + 1]); v[1] = pk2(acc[b][4 * g + 2], acc[b][4 * g + 3]);
                    const int eb = (e0 >> 4) + (g >> 1);
                    *(u32x2*)(yp + ((size_t)(tb * 40 + eb) * 64 + (t & 15) + 16 * (2 * (g & 1) + h)) * 8) = v;
                }
            }
        }
    }
    STAMP(a.ws, 1, 3);
    WAIT_LGKM0();
    BARRIER();
    char* ot = lds + 65536;
    int* badf = (int*)(lds + 65536 + 8 * 1296);
    if (tid == 0) *badf = 0;
    {
        f32x4* ex = (f32x4*)lds + (wid * 3) * 64 + lane;
#pragma unroll
        for (int g = 0; g < 3; ++g) {
            f32x4 t;
#pragma unroll
            for (int j = 0; j < 4; ++j) t[j] = kh ? o[0][4 * g + j] : o[1][4 * g + j];
            ex[g * 64] = t;
        }
    }
    __syncthreads();
    int bad = 0;
    {
        const f32x4* ex = (const f32x4*)lds + ((wid ^ 1) * 3) * 64 + lane;
        float v[12];
#pragma unroll
        for (int g = 0; g < 3; ++g) { const f32x4 t = ex[g * 64];
#pragma unroll
            for (int j = 0; j < 4; ++j) v[4 * g + j] = (kh ? o[1][4 * g + j] : o[0][4 * g + j]) + t[j]; }
        const auto rr = __builtin_amdgcn_permlane32_swap(__float_as_uint(v[8]), __float_as_uint(v[8]), false, false);
        const float l = __uint_as_float(rr[1]);
        const float inv = 1.0f / l;
        bad = !(l >= 8.271806e-25f && l < 3.0e38f);
#pragma unroll
        for (int j = 0; j < 12; ++j) if (j < 8 || h == 0) bad |= !(fabsf(v[j]) < 3.0e38f);
        char* dst = ot + (2 * rg + kh) * 1296 + (r * 20 + 4 * h) * 2;
#pragma unroll
        for (int g = 0; g < 3; ++g) {
            if (g < 2 || h == 0) { u32x2 w; w[0] = pk2(v[4 * g] * inv, v[4 * g + 1] * inv); w[1] = pk2(v[4 * g + 2] * inv, v[4 * g + 3] * inv); *(u32x2*)(dst + g * 16) = w; }
        }
    }
    if (bad) *badf = 1;
    __syncthreads();
    if (__builtin_amdgcn_readfirstlane(*badf)) {
        const u16* qp = (const u16*)(a.ws + WS_QP);
        const int row = c * 256 + wid * 32 + r;
        const u32x4 f1 = *(const u32x4*)(qp + (size_t)row * 20 + 8 * h);
        const u32x2 f2 = *(const u32x2*)(qp + (size_t)row * 20 + 16);
        u32x4 g2; g2[0] = h ? 0u : f2[0]; g2[1] = h ? 0u : f2[1]; g2[2] = 0u; g2[3] = 0u;
        const bf16x8 fq1 = __builtin_bit_cast(bf16x8, f1), fq2 = __builtin_bit_cast(bf16x8, g2);
        f32x16 oo = {};
        float mrun = -3.0e38f;
        const char* kv = a.ws + WS_KV;
#pragma unroll 1
        for (int T = 0; T < 64; ++T) {
            const char* b = kv + (T >> 2) * KV_STAGE + (T & 3) * KV_TILE;
            const bf16x8 k1 = *(const bf16x8*)(b + h * 512 + r * 16), k2 = *(const bf16x8*)(b + 1024 + r * 16);
            const bf16x8 vf0 = *(const bf16x8*)(b + 1536 + (h * 21 + dcl) * 16), vf1 = *(const bf16x8*)(b + 1536 + ((2 + h) * 21 + dcl) * 16);
            f32x16 s = MFMA32(k1, fq1, zero);
            s = MFMA32(k2, fq2, s);
            float mx = s[0];
#pragma unroll
            for (int j = 1; j < 16; ++j) mx = fmaxf(mx, s[j]);
            mx = fmaxf(mx, __shfl_xor(mx, 32));
            const float mn = fmaxf(mrun, mx);
            const float al = __builtin_amdgcn_exp2f(mrun - mn);
            mrun = mn;
#pragma unroll
            for (int j = 0; j < 16; ++j) oo[j] *= al;
            u32x4 p0, p1;
#pragma unroll
            for (int j = 0; j < 4; ++j) {
                p0[j] = pk2(__builtin_amdgcn_exp2f(s[2 * j] - mn), __builtin_amdgcn_exp2f(s[2 * j + 1] - mn));
                p1[j] = pk2(__builtin_amdgcn_exp2f(s[8 + 2 * j] - mn), __builtin_amdgcn_exp2f(s[8 + 2 * j + 1] - mn));
            }
            oo = MFMA32(vf0, __builtin_bit_cast(bf16x8, p0), oo);
            oo = MFMA32(vf1, __builtin_bit_cast(bf16x8, p1), oo);
        }
        const float l = __shfl(oo[8], r + 32);
        const float inv = 1.0f / l;
        const int rr = wid * 32 + r;
        unsigned* dst = (unsigned*)(ot + (rr >> 5) * 1296 + (rr & 31) * 40);
#pragma unroll
        for (int g = 0; g < 3; ++g) {
            const int d0 = 8 * g + 4 * h;
            if (d0 < 20) { dst[d0 / 2] = pk2(oo[4 * g] * inv, oo[4 * g + 1] * inv); dst[d0 / 2 + 1] = pk2(oo[4 * g + 2] * inv, oo[4 * g + 3] * inv); }
        }
        __syncthreads();
    }
    {
        char* of = a.ws + WS_OF;
        const int tb = c >> 1;
#pragma unroll
        for (int p = 0; p < 2; ++p) {
            const int i = tid + p * 512;
            if (i < 640) {
                const int sq = i & 7, kc = i >> 3;
                const u32x4 v = *(const u32x4*)(ot + sq * 1296 + kc * 16);
                *(u32x4*)(of + ((size_t)(tb * 20 + (kc >> 2)) * 64 + 8 * (c & 1) + sq + 16 * (kc & 3)) * 16) = v;
            }
        }
    }
    STAMP(a.ws, 1, 4); STAMPR(a.ws, 1, 7);
}

constexpr int K3_EX = 102400, K3_OT = K3_EX + 8 * 3 * 1024, K3_LDS = K3_OT + 64 * 176;
struct K3Args {
    char* ws;
    u16* out;
};
__global__ __launch_bounds__(512) void k3_out(K3Args a) {
    extern __shared__ __attribute__((aligned(16))) char lds[];
    const int tid = threadIdx.x, lane = tid & 63, wid = __builtin_amdgcn_readfirstlane(tid >> 6);
    const unsigned lds0 = (unsigned)(uintptr_t)(LDSP char*)lds;
    const int xcd = blockIdx.x & 7, idx = blockIdx.x >> 3;
    const int mt = xcd * 4 + (idx >> 3), nt = idx & 7;
    STAMP(a.ws, 2, 0); STAMPR(a.ws, 2, 6);
    const int tw = wid & 3, kh = wid >> 2;
    const int tb = mt * 4 + tw;
    bf16x8 of[10];
    const char* ofp = a.ws + WS_OF + ((size_t)tb * 20 + kh * 10) * 1024 + lane * 16;
#pragma unroll
    for (int s = 0; s < 5; ++s) of[s] = *(const bf16x8*)(ofp + s * 1024);
    const __amdgpu_buffer_rsrc_t rW = mk_rsrc(a.ws + WS_WF, D * D * 2);
#pragma unroll
    for (int ph = 0; ph < 2; ++ph) {
#pragma unroll
        for (int j = 0; j < 7; ++j) {
            int i = wid + 8 * j; i = i < 50 ? i : i - 8;
            const int blk = i / 10, sq = i % 10, step = (sq / 5) * 10 + ph * 5 + sq % 5;
            dma16(rW, lane * 16, nt * 102400 + (blk * 20 + step) * 1024, lds0 + (blk * 20 + step) * 1024);
        }
        if (ph == 0) {
#pragma unroll
            for (int s = 5; s < 10; ++s) of[s] = *(const bf16x8*)(ofp + s * 1024);
        }
    }
    u32x2 yp[4][3];
#pragma unroll
    for (int p = 0; p < 4; ++p)
#pragma unroll
        for (int b = 0; b < 3; ++b) { const int bb = kh ? (b < 2 ? 3 + b : 3) : b; yp[p][b] = *(const u32x2*)(a.ws + WS_YP + (size_t)p * S * D * 2 + ((size_t)(tb * 40 + nt * 5 + bb) * 64 + lane) * 8); }
    const float g = ((const float*)(a.ws + WS_G))[tb * 16 + (lane & 15)];
    f32x4 acc[5];
#pragma unroll
    for (int b = 0; b < 5; ++b) acc[b] = f32x4{0.f, 0.f, 0.f, 0.f};
    WAIT_VM(25);
    BARRIER();
    STAMP(a.ws, 2, 1);
    const unsigned wb = lds0 + kh * 10 * 1024 + lane * 16;
    bf16x8 wf[2][5];
#pragma unroll
    for (int b = 0; b < 5; ++b) wf[0][b] = lds_ld8(wb + (b * 20) * 1024);
#pragma unroll
    for (int s = 0; s < 10; ++s) {
        if (s == 4) { WAIT_VM(13); BARRIER(); }
        if (s + 1 < 10) {
#pragma unroll
            for (int b = 0; b < 5; ++b) wf[(s + 1) & 1][b] = lds_ld8(wb + (b * 20 + s + 1) * 1024);
        }
        SBAR();
#pragma unroll
        for (int b = 0; b < 5; ++b) acc[b] = MFMA16(wf[s & 1][b], of[s], acc[b]);
        SBAR();
    }
    STAMP(a.ws, 2, 2);
    {
        f32x4* ex = (f32x4*)(lds + K3_EX) + (wid * 3) * 64 + lane;
        if (kh == 0) { ex[0] = acc[3]; ex[64] = acc[4]; } else { ex[0] = acc[0]; ex[64] = acc[1]; ex[128] = acc[2]; }
    }
    __syncthreads();
    char* ot = lds + K3_OT;
    {
        const f32x4* ex = (const f32x4*)(lds + K3_EX) + ((wid ^ 4) * 3) * 64 + lane;
#pragma unroll
        for (int b = 0; b < 3; ++b) {
            if (kh == 0 || b < 2) {
                const f32x4 t = ex[b * 64];
                const f32x4 mine = kh ? acc[3 + (b < 2 ? b : 0)] : acc[b];
                float y[4];
#pragma unroll
                for (int j = 0; j < 4; ++j) {
                    float sum = 0.f;
#pragma unroll
                    for (int p = 0; p < 4; ++p) sum += (j & 1) ? __uint_as_float(yp[p][b][j >> 1] & 0xffff0000u) : __uint_as_float(yp[p][b][j >> 1] << 16);
                    y[j] = g * (mine[j] + t[j]) + (1.0f - g) * sum;
                }
                u32x2 v; v[0] = pk2(y[0], y[1]); v[1] = pk2(y[2], y[3]);
                *(u32x2*)(ot + (tw * 16 + (lane & 15)) * 176 + (16 * (kh * 3 + b) + 4 * (lane >> 4)) * 2) = v;
            }
        }
    }
    __syncthreads();
#pragma unroll
    for (int p = 0; p < 2; ++p) {
        const int i = tid + p * 512;
        if (i < 640) {
            const int row = i / 10, ch = i % 10;
            const u32x4 v = *(const u32x4*)(ot + row * 176 + ch * 16);
            *(u32x4*)(a.out + (size_t)(mt * 64 + row) * D + nt * 80 + ch * 8) = v;
        }
    }
    STAMP(a.ws, 2, 4); STAMPR(a.ws, 2, 7);
}

extern "C" void kernel_launch(void* const* d_in, const int* in_sizes, int n_in, void* d_out, int out_size, void* d_ws, size_t ws_size, hipStream_t stream) {
    (void)in_sizes; (void)n_in; (void)out_size; (void)ws_size;
    (void)hipFuncSetAttribute((const void*)k1_proj, hipFuncAttributeMaxDynamicSharedMemorySize, K1_LDS);
    (void)hipFuncSetAttribute((const void*)k2_main, hipFuncAttributeMaxDynamicSharedMemorySize, K2_LDS);
    (void)hipFuncSetAttribute((const void*)k3_out, hipFuncAttributeMaxDynamicSharedMemorySize, K3_LDS);
    K1Args a1;
    a1.xa = (const u16*)d_in[0]; a1.wq = (const u16*)d_in[1]; a1.wk = (const u16*)d_in[2]; a1.wv = (const u16*)d_in[3]; a1.wo = (const u16*)d_in[4];
    a1.wr = (const u16*)d_in[8]; a1.wi = (const u16*)d_in[9]; a1.gw = (const u16*)d_in[10];
    a1.ws = (char*)d_ws;
    hipLaunchKernelGGL(k1_proj, dim3(256), dim3(512), K1_LDS, stream, a1);
    K2Args a2; a2.cre = (const u16*)d_in[6]; a2.cim = (const u16*)d_in[7]; a2.ws = (char*)d_ws;
    hipLaunchKernelGGL(k2_main, dim3(256), dim3(512), K2_LDS, stream, a2);
    K3Args a3; a3.ws = (char*)d_ws; a3.out = (u16*)d_out;
    hipLaunchKernelGGL(k3_out, dim3(256), dim3(512), K3_LDS, stream, a3);
}
```

```cpp
#include <hip/hip_runtime.h>
#include <hip/hip_bf16.h>
#include <stdint.h>
#include <type_traits>

typedef __bf16 bf16x8 __attribute__((ext_vector_type(8)));
typedef __bf16 bf16x4 __attribute__((ext_vector_type(4)));
typedef __bf16 bf16x2 __attribute__((ext_vector_type(2)));
typedef short s16x4 __attribute__((ext_vector_type(4)));
typedef float f32x16 __attribute__((ext_vector_type(16)));
typedef float f32x4 __attribute__((ext_vector_type(4)));
typedef float f32x2 __attribute__((ext_vector_type(2)));
typedef unsigned u32x4 __attribute__((ext_vector_type(4)));
typedef unsigned u32x2 __attribute__((ext_vector_type(2)));
typedef unsigned short u16;
#define LDSP __attribute__((address_space(3)))
#define DI __device__ __forceinline__
#define MFMA32(a, b, c) __builtin_amdgcn_mfma_f32_32x32x16_bf16((a), (b), (c), 0, 0, 0)
#define MFMA16(a, b, c) __builtin_amdgcn_mfma_f32_16x16x32_bf16((a), (b), (c), 0, 0, 0)
#define SBAR() __builtin_amdgcn_sched_barrier(0)
#define K2_CODE_LINES 192

constexpr int S = 2048, D = 640, NH = 32, DH = 20;
constexpr float QSCALE = 0.22360679774997896f * 1.4426950408889634f;

constexpr size_t WS_QP = 0;
constexpr size_t WS_KV = WS_QP + (size_t)S * D * 2;
constexpr int KV_STAGE = 12288, KV_TILE = 2944;
constexpr size_t WS_BT = WS_KV + 16 * KV_STAGE;
constexpr size_t WS_WF = WS_BT + (size_t)1280 * S * 2;
constexpr size_t WS_G = WS_WF + (size_t)D * D * 2;
constexpr size_t WS_KM = WS_G + S * 4;
constexpr size_t WS_OF = WS_KM + 256;
constexpr size_t WS_YP = WS_OF + (size_t)S * D * 2;
constexpr size_t WS_CNT = WS_YP + (size_t)4 * S * D * 2;
constexpr int NCNT = 97;
constexpr size_t WS_END = WS_CNT + NCNT * 128;

DI unsigned pk2(float a, float b) { f32x2 v = {a, b}; bf16x2 r = __builtin_convertvector(v, bf16x2); return __builtin_bit_cast(unsigned, r); }
DI float bf2f(u16 x) { return __uint_as_float((unsigned)x << 16); }
DI __amdgpu_buffer_rsrc_t mk_rsrc(const void* p, unsigned bytes) { return __builtin_amdgcn_make_buffer_rsrc((void*)p, (short)0, (int)bytes, 0x00020000); }
DI void dma16(__amdgpu_buffer_rsrc_t rs, unsigned voff, unsigned soff, unsigned lds) {
    asm volatile("s_mov_b32 m0, %3\n\ts_nop 0\n\tbuffer_load_dwordx4 %0, %1, %2 offen lds" ::"v"(voff), "s"(rs), "s"(soff), "s"(lds) : "memory");
}
#define WAIT_VM(n) asm volatile("s_waitcnt vmcnt(" #n ")" ::: "memory")
#define WAIT_LGKM0() asm volatile("s_waitcnt lgkmcnt(0)" ::: "memory")
#define BARRIER() asm volatile("s_barrier" ::: "memory")
DI bf16x8 lds_ld8(unsigned a) { return *(const LDSP bf16x8*)(uintptr_t)a; }
#define LLD(dst, a) dst = lds_ld8(a)
DI s16x4 lds_tr(unsigned a) { return __builtin_amdgcn_ds_read_tr16_b64_v4i16((LDSP s16x4*)(uintptr_t)a); }

constexpr int K1_STAGE = 32768, K1_NS = 5, K1_LDS = K1_STAGE * K1_NS;
struct K2Args {
    const u16 *cre, *cim;
    char* ws;
    u16* out;
};
struct K1Args {
    const u16 *xa, *wq, *wk, *wv, *wo, *wr, *wi, *gw, *cre, *cim;
    char* ws;
};
DI void touch(unsigned lds_scratch, const void* p) { asm volatile("s_mov_b32 m0, %1\n\ts_nop 0\n\tglobal_load_lds_dword %0, off" ::"v"(p), "s"(lds_scratch) : "memory"); }
DI void warm_next(unsigned lds_scratch, const u16* cre, const u16* cim, int wid, int lane) {
    if (wid < 1) {
        const int L = (wid * 256 + blockIdx.x) * 64 + lane;
        const int g = L >> 13, q = L & 8191;
        touch(lds_scratch, (const char*)((q >> 12) ? cim : cre) + (size_t)(q & 2047) * 4096 + ((q >> 11) & 1) * 2048 + g * 128);
    } else if (wid == 7 && blockIdx.x * 64 < K2_CODE_LINES) {
        unsigned c2lo, c2hi;
        asm volatile("s_getpc_b64 s[90:91]\n\ts_add_u32 %0, s90, _Z7k2_main6K2Args@rel32@lo+4\n\ts_addc_u32 %1, s91, _Z7k2_main6K2Args@rel32@hi+12" : "=s"(c2lo), "=s"(c2hi) :: "s90", "s91", "scc");
        int ln = blockIdx.x * 64 + lane; ln = ln < K2_CODE_LINES ? ln : 0;
        touch(lds_scratch, (const char*)(((unsigned long long)c2hi << 32) | c2lo) + ln * 128);
    }
}
template <int N> DI void wait_vm() { asm volatile("s_waitcnt vmcnt(%0)" ::"n"(N) : "memory"); }

template <int MODE>
DI void k1_gemm(f32x16 (&acc)[2], unsigned lds0, __amdgpu_buffer_rsrc_t rx, unsigned xrow0, __amdgpu_buffer_rsrc_t rw, unsigned wbase, int wid, int lane) {
    const unsigned xr = lane >> 3, xc = lane & 7;
    const unsigned xv0 = (16 * wid + xr) * 1280 + ((xc ^ (xr >> 1)) << 4);
    const unsigned xv1 = (16 * wid + 8 + xr) * 1280 + ((xc ^ (4 + (xr >> 1))) << 4);
    unsigned wv0, wv1;
    if (MODE == 0) {
        const unsigned kr = lane >> 4, cc = lane & 15;
        wv0 = (8 * wid + kr) * 1280 + (((((cc >> 2) ^ kr) << 2) | (cc & 3)) << 4);
        wv1 = wv0 + 4 * 1280;
    } else {
        wv0 = xv0; wv1 = xv1;
    }
    auto piece = [&](int st, int i) {
        const unsigned sl = lds0 + (st % K1_NS) * K1_STAGE + wid * 2048;
        const unsigned xs = xrow0 * 1280 + st * 128;
        const unsigned wsb = wbase + (MODE == 0 ? st * 64 * 1280 : st * 128);
        if (i == 0) dma16(rx, xv0, xs, sl); else if (i == 1) dma16(rw, wv0, wsb, sl + 16384);
        else if (i == 2) dma16(rx, xv1, xs, sl + 1024); else dma16(rw, wv1, wsb, sl + 16384 + 1024);
    };
#pragma unroll
    for (int st = 0; st < 4; ++st)
#pragma unroll
        for (int i = 0; i < 4; ++i) piece(st, i);
    const int r = lane & 31, h = lane >> 5;
    const int wi = wid >> 1, wj = wid & 1;
    const unsigned sw = (r >> 1) & 7;
    const unsigned tq = (lane & 15) >> 2;
    unsigned xo[4], wo_;
#pragma unroll
    for (int ks = 0; ks < 4; ++ks) xo[ks] = r * 128 + (((2 * ks + h) ^ sw) << 4);
    const unsigned xblk = (MODE == 0) ? wj * 64 * 128 : wi * 32 * 128;
    if (MODE == 0) wo_ = 16384 + (8 * h + tq) * 256 + ((lane >> 4) & 1) * 32 + (lane & 3) * 8 + ((wi ^ tq) << 6);
    else wo_ = 16384 + wj * 64 * 128;
    bf16x8 f0[2][4], f1[2][4], f2[2][4];
    auto frag = [&](int st, int ks) {
        const unsigned sl = lds0 + (st % K1_NS) * K1_STAGE;
        const int q = st & 1;
        if (MODE == 0) {
            const s16x4 lo = lds_tr(sl + wo_ + ks * 4096), hi = lds_tr(sl + wo_ + ks * 4096 + 1024);
            f0[q][ks] = __builtin_bit_cast(bf16x8, __builtin_shufflevector(lo, hi, 0, 1, 2, 3, 4, 5, 6, 7));
            f1[q][ks] = lds_ld8(sl + xblk + xo[ks]); f2[q][ks] = lds_ld8(sl + xblk + 4096 + xo[ks]);
        } else {
            f0[q][ks] = lds_ld8(sl + xblk + xo[ks]);
            f1[q][ks] = lds_ld8(sl + wo_ + xo[ks]); f2[q][ks] = lds_ld8(sl + wo_ + 4096 + xo[ks]);
        }
    };
    wait_vm<12>();
    BARRIER();
#pragma unroll
    for (int ks = 0; ks < 4; ++ks) frag(0, ks);
    SBAR();
#pragma unroll
    for (int st = 0; st < 10; ++st) {
        if (st + 1 < 10) {
            if (st <= 6) wait_vm<8>(); else if (st == 7) wait_vm<4>(); else wait_vm<0>();
            WAIT_LGKM0();
            BARRIER();
        }
#pragma unroll
        for (int ks = 0; ks < 4; ++ks) {
            if (st + 1 < 10) frag(st + 1, ks);
            SBAR();
            acc[0] = MFMA32(f0[st & 1][ks], f1[st & 1][ks], acc[0]); acc[1] = MFMA32(f0[st & 1][ks], f2[st & 1][ks], acc[1]);
            SBAR();
            if (st + 4 < 10) piece(st + 4, ks);
            SBAR();
        }
    }
    WAIT_LGKM0();
    BARRIER();
}

DI void k1_store_tile(const f32x16 (&acc)[2], float scale, char* lds, u16* dst, int pitch_elems, int tid, int wid, int lane) {
    const int r = lane & 31, h = lane >> 5, wi = wid >> 1, wj = wid & 1;
#pragma unroll
    for (int bj = 0; bj < 2; ++bj)
#pragma unroll
        for (int g = 0; g < 4; ++g) {
            u32x2 v; v[0] = pk2(acc[bj][4 * g] * scale, acc[bj][4 * g + 1] * scale); v[1] = pk2(acc[bj][4 * g + 2] * scale, acc[bj][4 * g + 3] * scale);
            const int row = wj * 64 + bj * 32 + r, col = wi * 32 + 8 * g + 4 * h;
            *(u32x2*)(lds + row * 272 + col * 2) = v;
        }
    __syncthreads();
    const __amdgpu_buffer_rsrc_t rd = mk_rsrc(dst, (127 * pitch_elems + 128) * 2);
#pragma unroll
    for (int p = 0; p < 4; ++p) {
        const int row = p * 32 + (tid >> 4), ch = tid & 15;
        const u32x4 v = *(const u32x4*)(lds + row * 272 + ch * 16);
        __builtin_amdgcn_raw_buffer_store_b128(v, rd, (row * pitch_elems + ch * 8) * 2, 0, 16);
    }
}

__global__ __launch_bounds__(512, 2) void k1_proj(K1Args a) {
    extern __shared__ __attribute__((aligned(16))) char lds[];
    const int tid = threadIdx.x, lane = tid & 63, wid = __builtin_amdgcn_readfirstlane(tid >> 6);
    const int xcd = blockIdx.x & 7, idx = blockIdx.x >> 3;
    const int mb = 4 * (xcd >> 1) + (idx >> 3), t = 8 * (xcd & 1) + (idx & 7);
    if (blockIdx.x < NCNT && tid == 0) *(unsigned*)(a.ws + WS_CNT + blockIdx.x * 128) = 0u;
    const unsigned lds0 = (unsigned)(uintptr_t)(LDSP char*)lds;
    warm_next(lds0 + K1_LDS - 256, a.cre, a.cim, wid, lane);
    f32x16 acc[2];
#pragma unroll
    for (int k = 0; k < 16; ++k) { acc[0][k] = 0.f; acc[1][k] = 0.f; }
    if (t < 5) {
        k1_gemm<0>(acc, lds0, mk_rsrc(a.xa, S * D * 2), mb * 128, mk_rsrc(a.wq, D * D * 2), t * 256, wid, lane);
        k1_store_tile(acc, QSCALE, lds, (u16*)(a.ws + WS_QP) + (size_t)mb * 128 * D + t * 128, D, tid, wid, lane);
    } else if (t >= 6) {
        const int eb = t - 6;
        const int pair = mb * 10 + eb, nt = pair / 20, step = pair % 20;
        u16 wv[8];
        {
            const int ch = tid < 320 ? tid : 0;
            const int e = 80 * nt + 16 * (ch >> 6) + (ch & 15), k0 = 32 * step + 8 * ((ch & 63) >> 4);
#pragma unroll
            for (int j = 0; j < 8; ++j) wv[j] = a.wo[(size_t)(k0 + j) * D + e];
        }
        const u16* w = eb < 5 ? a.wr : a.wi;
        k1_gemm<1>(acc, lds0, mk_rsrc(a.xa, S * D * 2), mb * 128, mk_rsrc(w, D * D * 2), (eb < 5 ? eb : eb - 5) * 128 * 1280, wid, lane);
        if (tid < 320) {
            u32x4 v;
#pragma unroll
            for (int j = 0; j < 4; ++j) v[j] = (unsigned)wv[2 * j] | ((unsigned)wv[2 * j + 1] << 16);
            __builtin_amdgcn_raw_buffer_store_b128(v, mk_rsrc(a.ws + WS_WF, D * D * 2), (nt * 100 + ((tid >> 6) * 10 + step % 10) * 2 + step / 10) * 1024 + (tid & 63) * 16, 0, 16);
        }
        k1_store_tile(acc, eb < 5 ? 1.f : -1.f, lds, (u16*)(a.ws + WS_BT) + (size_t)eb * 128 * S + mb * 128, S, tid, wid, lane);
    } else {
        const __amdgpu_buffer_rsrc_t rx = mk_rsrc(a.xa, S * D * 2);
        const unsigned xr = lane >> 3, xc = lane & 7;
        const unsigned xv0 = (16 * wid + xr) * 1280 + ((xc ^ (xr >> 1)) << 4);
        const unsigned xv1 = (16 * wid + 8 + xr) * 1280 + ((xc ^ (4 + (xr >> 1))) << 4);
        const unsigned xl0 = lds0 + 81920;
        u32x2 wl[2][10]; u16 gl[2];
#pragma unroll
        for (int p = 0; p < 2; ++p) {
            const int k = (tid + p * 512) < 640 ? tid + p * 512 : 0;
#pragma unroll
            for (int c = 0; c < 5; ++c) { wl[p][c] = *(const u32x2*)(a.wk + k * 20 + c * 4); wl[p][5 + c] = *(const u32x2*)(a.wv + k * 20 + c * 4); }
            gl[p] = a.gw[k];
        }
        auto issue = [&](int st) {
            const unsigned sl = xl0 + (st & 3) * 16384 + wid * 2048;
            dma16(rx, xv0, mb * 128 * 1280 + st * 128, sl); dma16(rx, xv1, mb * 128 * 1280 + st * 128, sl + 1024);
        };
        issue(0); issue(1); issue(2);
#pragma unroll
        for (int p = 0; p < 2; ++p) {
            const int k = tid + p * 512;
            if (k < 640) {
                char* row = lds + k * 128;
                const int sx = ((k >> 1) & 1) << 6;
#pragma unroll
                for (int c = 0; c < 10; ++c) *(u32x2*)(row + ((c * 8) ^ sx)) = wl[p][c];
                *(u16*)(row + (80 ^ sx)) = gl[p];
            }
        }
        const int r = lane & 31, h = lane >> 5;
        const int sb = wid >> 1, nb = wid & 1;
        const unsigned sw = (r >> 1) & 7, tq = (lane & 15) >> 2;
        const unsigned wofs = (8 * h + tq) * 128 + ((nb ^ ((tq >> 1) & 1)) << 6) + ((lane >> 4) & 1) * 32 + (lane & 3) * 8;
        f32x16 c0 = acc[0];
#pragma unroll
        for (int st = 0; st < 10; ++st) {
            if (st <= 7) wait_vm<4>(); else if (st == 8) wait_vm<2>(); else wait_vm<0>();
            WAIT_LGKM0();
            BARRIER();
            if (st + 3 < 10) issue(st + 3);
            const unsigned sl = xl0 + (st & 3) * 16384;
            bf16x8 xf[4], wf[4];
#pragma unroll
            for (int ks = 0; ks < 4; ++ks) {
                xf[ks] = lds_ld8(sl + sb * 4096 + r * 128 + (((2 * ks + h) ^ sw) << 4));
                const unsigned wa = lds0 + (st * 64 + ks * 16) * 128 + wofs;
                const s16x4 lo = lds_tr(wa), hi = lds_tr(wa + 512);
                wf[ks] = __builtin_bit_cast(bf16x8, __builtin_shufflevector(lo, hi, 0, 1, 2, 3, 4, 5, 6, 7));
            }
            SBAR();
#pragma unroll
            for (int ks = 0; ks < 4; ++ks) c0 = MFMA32(wf[ks], xf[ks], c0);
            SBAR();
        }
        WAIT_LGKM0();
        BARRIER();
        float* T = (float*)lds;
#pragma unroll
        for (int g = 0; g < 4; ++g) {
            f32x4 v0 = {c0[4 * g], c0[4 * g + 1], c0[4 * g + 2], c0[4 * g + 3]};
            *(f32x4*)(T + (sb * 32 + r) * 68 + nb * 32 + 8 * g + 4 * h) = v0;
        }
        __syncthreads();
        char* img = a.ws + WS_KV + (size_t)mb * KV_STAGE;
        if (tid < 384) {
            const int i = tid, tt = i / 96, c = (i % 96) >> 5, key = i & 31;
            const float* row = T + (tt * 32 + key) * 68;
            u32x4 v;
            if (c < 2) { v[0] = pk2(row[8 * c], row[8 * c + 1]); v[1] = pk2(row[8 * c + 2], row[8 * c + 3]); v[2] = pk2(row[8 * c + 4], row[8 * c + 5]); v[3] = pk2(row[8 * c + 6], row[8 * c + 7]); }
            else { v[0] = pk2(row[16], row[17]); v[1] = pk2(row[18], row[19]); v[2] = 0x00003F80u; v[3] = 0u; }
            *(u32x4*)(img + tt * KV_TILE + c * 512 + key * 16) = v;
        }
        if (tid < 4 * 88) {
            const int i = tid, tt = i / 88, q = i % 88, sh = q / 22, d = q % 22, s2 = sh >> 1, hh = sh & 1;
            float e[8];
#pragma unroll
            for (int j = 0; j < 8; ++j) { const int key = 16 * s2 + 8 * (j >> 2) + 4 * hh + (j & 3); e[j] = d < 20 ? T[(tt * 32 + key) * 68 + 20 + d] : (d == 20 ? 1.0f : 0.0f); }
            u32x4 v; v[0] = pk2(e[0], e[1]); v[1] = pk2(e[2], e[3]); v[2] = pk2(e[4], e[5]); v[3] = pk2(e[6], e[7]);
            *(u32x4*)(img + tt * KV_TILE + 1536 + q * 16) = v;
        }
        float n2 = 0.f;
        if (tid >= 384) {
            const int rr = tid - 384;
            const float* row = T + rr * 68;
#pragma unroll
            for (int d = 0; d < 20; ++d) { const float kv = bf2f((u16)(pk2(row[d], 0.f) & 0xffff)); n2 += kv * kv; }
            ((float*)(a.ws + WS_G))[mb * 128 + rr] = 1.0f / (1.0f + __expf(-row[40]));
#pragma unroll
            for (int o = 32; o >= 1; o >>= 1) n2 = fmaxf(n2, __shfl_xor(n2, o));
            if (lane == 0) ((float*)(a.ws + WS_KM))[mb * 2 + (wid & 1)] = n2;
        }
    }
}

constexpr int K2_A = 0, K2_B = 3 * 16384, K2_KV = K2_B + 3 * 20480, K2_Q = K2_KV + 3 * KV_STAGE, K2_LDS = K2_Q + 11264;
#define PIN(x) asm volatile("" : "+v"(x))
#define EXP2(x) __builtin_amdgcn_exp2f(x)
struct K2Regs {
    f32x16 s[2];
    u32x4 p0, p1;
    bf16x8 kA1, kA2, kB1, kB2, vA0, vA1, vB0, vB1;
    bf16x8 af[2], bf[2][3];
};
#define EX(i) sc[i] = EXP2(sc[i])
#define CV0(i) p0[i] = pk2(sc[2 * (i)], sc[2 * (i) + 1])
#define CV1(i) p1[i] = pk2(sc[8 + 2 * (i)], sc[8 + 2 * (i) + 1])
#define GE() do { PIN(sc); SBAR(); } while (0)
#define GE0() do { PIN(sc); PIN(p0); SBAR(); } while (0)
#define GE1() do { PIN(sc); PIN(p1); SBAR(); } while (0)
struct K2Dma {
    __amdgpu_buffer_rsrc_t rA, rB, rK, rW, rY;
    unsigned v0, v1, vk;
    unsigned sA, sB, sK;
    unsigned lA, lB, lK;
    unsigned vb0, vb1;
    unsigned sW, lds0;
    unsigned vY;
    int wid;
};
template <int NB, int I> DI void k2_dma_kv1(const K2Dma& d, int st, int sl) {
    if (I < (NB == 3 ? 1 : 2)) dma16(d.rK, d.vk, d.sK + st * KV_STAGE + I * 1024, d.lK + sl * KV_STAGE + I * 1024);
}
template <int NB, int I> DI void k2_dma_ab1(const K2Dma& d, int st, int sl) {
    if (I < 2 + NB) {
        if (I < 2) dma16(d.rA, I ? d.v1 : d.v0, d.sA + st * 128 + I * 8 * 4096, d.lA + sl * 16384 + I * 1024);
        else dma16(d.rB, ((I - 2) & 1) ? d.vb1 : d.vb0, d.sB + st * 128 + (I - 2) * 8 * 4096, d.lB + sl * 20480 + (I - 2) * 1024);
    }
}
template <int NB> DI void k2_dma_kv(const K2Dma& d, int st, int sl) { k2_dma_kv1<NB, 0>(d, st, sl); k2_dma_kv1<NB, 1>(d, st, sl); }
template <int NB> DI void k2_dma_ab(const K2Dma& d, int st, int sl) { k2_dma_ab1<NB, 0>(d, st, sl); k2_dma_ab1<NB, 1>(d, st, sl); k2_dma_ab1<NB, 2>(d, st, sl); k2_dma_ab1<NB, 3>(d, st, sl); k2_dma_ab1<NB, 4>(d, st, sl); }
struct K2Addr { unsigned ga[4], gb[4], kb1, kb2, vb; };
DI constexpr unsigned k2_wf_addr(int i) {
    return i < 16 ? K2_A + 16384 + i * 1024 : (i < 36 ? K2_B + 20480 + (i - 16) * 1024 : (i < 52 ? K2_A + 32768 + (i - 36) * 1024 : (i < 72 ? K2_B + 40960 + (i - 52) * 1024 :
           (i < 88 ? K2_A + (i - 72) * 1024 : K2_B + (i - 88) * 1024))));
}
DI void k2_dma_wf(const K2Dma& d, int i) { dma16(d.rW, d.vk, d.sW + i * 1024, d.lds0 + k2_wf_addr(i)); }
DI void k2_ystore(const K2Dma& d, const f32x16& acc, int b) {
#pragma unroll
    for (int g = 0; g < 2; ++g) {
        u32x4 v; v[0] = pk2(acc[4 * g], acc[4 * g + 1]); v[1] = pk2(acc[4 * g + 2], acc[4 * g + 3]);
        v[2] = pk2(acc[8 + 4 * g], acc[8 + 4 * g + 1]); v[3] = pk2(acc[8 + 4 * g + 2], acc[8 + 4 * g + 3]);
        __builtin_amdgcn_raw_buffer_store_b128(v, d.rY, d.vY + b * 1024 + g * 512, 0, 16);
    }
}

template <int NB, int SL, int U, int DM>
DI void k2_unit(K2Regs& R, f32x16 (&o)[2], f32x16 (&acc)[3], const bf16x8 (&q1)[2], const bf16x8 (&q2)[2], const K2Addr& A, const K2Dma& d, int st) {
    constexpr int SN = (SL + 1) % 3, S2 = (SL + 2) % 3;
    constexpr int NKV = NB == 3 ? 1 : 2;
    constexpr bool GM = DM != 5 || U == 0, GL = DM != 5;
    f32x16& sc = R.s[U & 1]; f32x16& sn = R.s[(U + 1) & 1];
    u32x4 &p0 = R.p0, &p1 = R.p1;
    f32x16& ocur = o[U & 1]; f32x16& oprev = o[(U + 1) & 1];
    const bf16x8& kn1 = (U == 0 || U == 3) ? R.kA1 : R.kB1; const bf16x8& kn2 = (U == 0 || U == 3) ? R.kA2 : R.kB2;
    const bf16x8& qn1 = q1[(U + 1) & 1]; const bf16x8& qn2 = q2[(U + 1) & 1];
    const bf16x8& vprev1 = (U == 0 || U == 3) ? R.vB1 : R.vA1;
    const bf16x8& vcur0 = (U < 2) ? R.vA0 : R.vB0;
    const bf16x8& af = R.af[(U + 1) & 1]; const bf16x8 (&bf)[3] = R.bf[(U + 1) & 1];
    bf16x8& afn = R.af[U & 1]; bf16x8 (&bfn)[3] = R.bf[U & 1];
    const f32x16 zero = {};
    auto ld_a = [&]() {
        if (U == 0) LLD(R.kB1, A.kb1 + SL * KV_STAGE + KV_TILE);
        if (U == 1) LLD(R.vB0, A.vb + SL * KV_STAGE + KV_TILE);
        if (U == 2 && DM != 5) LLD(R.kA1, A.kb1 + SN * KV_STAGE);
        if (U == 3 && DM != 5) LLD(R.vA0, A.vb + SN * KV_STAGE);
        SBAR();
    };
    auto ld_b = [&]() {
        if (U == 0) LLD(R.kB2, A.kb2 + SL * KV_STAGE + KV_TILE);
        if (U == 1) LLD(R.vB1, A.vb + SL * KV_STAGE + KV_TILE + 704);
        if (U == 2 && DM != 5) LLD(R.kA2, A.kb2 + SN * KV_STAGE);
        if (U == 3 && DM != 5) LLD(R.vA1, A.vb + SN * KV_STAGE + 704);
        SBAR();
    };
    auto ld_g = [&](int i) {
        if (GL) { if (i == 0) LLD(afn, A.ga[U] + SN * 16384); else LLD(bfn[i - 1], A.gb[U] + SN * 20480 + (i - 1) * 4096); SBAR(); }
    };
    auto dm = [&](auto kc) {
        constexpr int k = decltype(kc)::value;
        constexpr int i = 2 * U + (k >> 1);
        if (DM == 1 && (k & 1) && U < 3) { if (i < NKV) k2_dma_kv1<NB, (i < NKV ? i : 0)>(d, st + 2, S2); else k2_dma_ab1<NB, (i >= NKV ? i - NKV : 0)>(d, st + 3, SL); SBAR(); }
        if (DM == 3 && (k & 1) && U < 3) { if (i < NKV) k2_dma_kv1<NB, (i < NKV ? i : 0)>(d, 15, S2); else k2_dma_wf(d, d.wid + 8 * (i - NKV)); SBAR(); }
        if (DM == 4 && (k & 1) && i < 6 - NKV) { k2_dma_wf(d, 36 + d.wid + 8 * i); SBAR(); }
        if (DM == 5 && (k & 1) && i < 5 - NKV) { k2_dma_wf(d, 72 + d.wid + 8 * i); SBAR(); }
        if (DM == 2) {
            constexpr int j = 4 * U + k;
            if (j < NKV) k2_dma_kv1<NB, (j < NKV ? j : 0)>(d, 2, 2);
            else if (j < NKV + 2 + NB) k2_dma_ab1<NB, (j >= NKV ? j - NKV : 0)>(d, 2, 2);
            else k2_dma_ab1<NB, (j >= NKV + 2 + NB ? j - NKV - 2 - NB : 0)>(d, 3, 0);
            SBAR();
        }
    };
    auto gm = [&](auto bc) {
        constexpr int b = decltype(bc)::value;
        if (GM) { acc[b] = MFMA32(bf[b], af, acc[b]); PIN(acc[b]); SBAR(); }
        else if (b == 0 && U - 1 < NB) { k2_ystore(d, acc[U > 0 ? U - 1 : 0], U - 1); SBAR(); }
    };
    using I0 = std::integral_constant<int, 0>; using I1 = std::integral_constant<int, 1>; using I2 = std::integral_constant<int, 2>; using I3 = std::integral_constant<int, 3>;
    __builtin_amdgcn_s_setprio(3 - U);
    sn = MFMA32(kn1, qn1, zero); PIN(sn); SBAR();
    EX(0); SBAR(); EX(1); SBAR(); EX(2); GE();
    dm(I0{});
    oprev = MFMA32(vprev1, __builtin_bit_cast(bf16x8, p1), oprev); PIN(oprev); SBAR();
    if (NB == 2) { EX(3); SBAR(); EX(4); SBAR(); EX(5); SBAR(); CV0(0); GE0(); } else { EX(3); SBAR(); EX(4); SBAR(); CV0(0); SBAR(); CV0(1); GE0(); }
    sn = MFMA32(kn2, qn2, sn); PIN(sn); SBAR();
    ld_a(); ld_b();
    if (NB == 2) { EX(6); SBAR(); EX(7); SBAR(); CV0(1); SBAR(); CV0(2); SBAR(); CV0(3); GE0(); } else { EX(5); SBAR(); EX(6); SBAR(); EX(7); GE(); }
    dm(I1{});
    gm(I0{});
    ld_g(0); ld_g(1);
    if (NB == 2) { EX(8); SBAR(); EX(9); SBAR(); EX(10); SBAR(); CV1(0); GE1(); } else { EX(8); SBAR(); EX(9); SBAR(); CV0(2); SBAR(); CV0(3); GE0(); EX(10); SBAR(); }
    ocur = MFMA32(vcur0, __builtin_bit_cast(bf16x8, p0), ocur); PIN(ocur); SBAR();
    ld_g(2);
    dm(I2{});
    if (NB == 2) { EX(11); SBAR(); EX(12); SBAR(); EX(13); SBAR(); CV1(1); GE1(); } else { EX(11); SBAR(); CV1(0); SBAR(); CV1(1); GE1(); }
    dm(I3{});
    gm(I1{});
    if (NB == 2) { EX(14); SBAR(); EX(15); SBAR(); CV1(2); SBAR(); CV1(3); GE1(); }
    else {
        EX(12); SBAR(); EX(13); SBAR(); EX(14); GE();
        gm(I2{});
        ld_g(3);
        EX(15); SBAR(); CV1(2); SBAR(); CV1(3); GE1();
    }
}
template <int NB, int SL, int DM>
DI void k2_stage(int st, K2Regs& R, f32x16 (&o)[2], f32x16 (&acc)[3], const bf16x8 (&q1)[2], const bf16x8 (&q2)[2], const K2Addr& A, const K2Dma& d) {
    if (DM == 2) { WAIT_VM(0); } else { if (NB == 3) WAIT_VM(5); else WAIT_VM(4); }
    BARRIER();
    SBAR();
    k2_unit<NB, SL, 0, DM>(R, o, acc, q1, q2, A, d, st);
    k2_unit<NB, SL, 1, DM>(R, o, acc, q1, q2, A, d, st);
    k2_unit<NB, SL, 2, DM>(R, o, acc, q1, q2, A, d, st);
    k2_unit<NB, SL, 3, DM>(R, o, acc, q1, q2, A, d, st);
}
template <int NB>
DI void k2_loop(f32x16 (&o)[2], f32x16 (&acc)[3], const bf16x8 (&q1)[2], const bf16x8 (&q2)[2], const K2Addr& A, const K2Dma& d) {
    K2Regs R;
    const f32x16 zero = {};
    const u32x4 z4 = {0u, 0u, 0u, 0u};
    R.kA1 = lds_ld8(A.kb1); R.kA2 = lds_ld8(A.kb2); R.vA0 = lds_ld8(A.vb); R.vA1 = lds_ld8(A.vb + 704);
    R.s[0] = MFMA32(R.kA1, q1[0], zero); R.s[0] = MFMA32(R.kA2, q2[0], R.s[0]);
    R.p1 = z4; R.p0 = z4; R.vB1 = R.vA1; R.vB0 = R.vA0; R.kB1 = R.kA1; R.kB2 = R.kA2;
    if (NB == 3) WAIT_VM(6); else WAIT_VM(6);
    BARRIER();
    {
        bf16x8 fa[3], fb[3][3];
#pragma unroll
        for (int ks = 0; ks < 3; ++ks) {
            fa[ks] = lds_ld8(A.ga[ks]);
#pragma unroll
            for (int b = 0; b < NB; ++b) fb[ks][b] = lds_ld8(A.gb[ks] + b * 4096);
        }
        R.af[1] = lds_ld8(A.ga[3]);
#pragma unroll
        for (int b = 0; b < 3; ++b) R.bf[1][b] = lds_ld8(A.gb[3] + (b < NB ? b : 0) * 4096);
        R.af[0] = R.af[1];
#pragma unroll
        for (int b = 0; b < 3; ++b) R.bf[0][b] = R.bf[1][b];
        SBAR();
#pragma unroll
        for (int ks = 0; ks < 3; ++ks)
#pragma unroll
            for (int b = 0; b < NB; ++b) acc[b] = MFMA32(fb[ks][b], fa[ks], acc[b]);
    }
    k2_stage<NB, 0, 2>(0, R, o, acc, q1, q2, A, d);
#pragma unroll 1
    for (int st = 1; st < 13; st += 3) {
        k2_stage<NB, 1, 1>(st, R, o, acc, q1, q2, A, d);
        k2_stage<NB, 2, 1>(st + 1, R, o, acc, q1, q2, A, d);
        k2_stage<NB, 0, 1>(st + 2, R, o, acc, q1, q2, A, d);
    }
    k2_stage<NB, 1, 3>(13, R, o, acc, q1, q2, A, d);
    k2_stage<NB, 2, 4>(14, R, o, acc, q1, q2, A, d);
    k2_stage<NB, 0, 5>(15, R, o, acc, q1, q2, A, d);
    __builtin_amdgcn_s_setprio(0);
    o[1] = MFMA32(R.vB1, __builtin_bit_cast(bf16x8, R.p1), o[1]);
}

typedef __attribute__((address_space(1))) unsigned gu32;
__global__ __launch_bounds__(512, 2) void k2_main(K2Args a) {
    extern __shared__ __attribute__((aligned(16))) char lds[];
    const int tid = threadIdx.x, lane = tid & 63, wid = __builtin_amdgcn_readfirstlane(tid >> 6);
    const int r = lane & 31, h = lane >> 5;
    const unsigned lds0 = (unsigned)(uintptr_t)(LDSP char*)lds;
    const int c = blockIdx.x, xcd = c & 7, idx = c >> 3;
    const int kq = xcd >> 1, mt = (xcd & 1) * 8 + (idx >> 2), nt = idx & 3;
    const int mi = wid & 3, nj = wid >> 2;
    const int rg = wid >> 1, kh = wid & 1;
    const int omt = xcd * 4 + (idx >> 3), ont = idx & 7;
    K2Dma d;
    d.rA = mk_rsrc((kq >> 1) ? a.cim : a.cre, S * S * 2);
    d.rB = mk_rsrc(a.ws + WS_BT, 1280 * S * 2);
    d.rK = mk_rsrc(a.ws + WS_KV, 16 * KV_STAGE);
    {
        const unsigned xr = lane >> 3, xc = lane & 7;
        d.v0 = xr * 4096 + ((xc ^ ((xr >> 1) & 7)) << 4); d.v1 = xr * 4096 + ((xc ^ ((4 + (xr >> 1)) & 7)) << 4); d.vk = lane * 16;
        const unsigned abase = (unsigned)(mt * 128) * 4096 + (kq & 1) * 2048;
        const unsigned bbase = (unsigned)((kq >> 1) * 640 + nt * 160) * 4096 + (kq & 1) * 2048;
        const int pb = wid < 4 ? 3 * wid : 12 + 2 * (wid - 4), pk = wid < 4 ? wid : 4 + 2 * (wid - 4);
        d.sA = abase + (2 * wid) * 8 * 4096; d.lA = lds0 + K2_A + 2 * wid * 1024;
        d.sB = bbase + pb * 8 * 4096; d.lB = lds0 + K2_B + pb * 1024;
        d.vb0 = (pb & 1) ? d.v1 : d.v0; d.vb1 = (pb & 1) ? d.v0 : d.v1;
        d.sK = pk * 1024; d.lK = lds0 + K2_KV + pk * 1024;
        d.rW = mk_rsrc(a.ws + WS_WF, D * D * 2); d.sW = ont * 102400; d.wid = wid; d.lds0 = lds0;
        d.rY = mk_rsrc(a.ws + WS_YP + (size_t)kq * S * D * 2, S * D * 2);
        const int t = mt * 128 + mi * 32 + r;
        d.vY = ((t >> 4) * 20 + nt * 5 + nj * 3) * 1024 + ((t & 15) + 16 * h) * 16;
    }
    const float* kmp = (const float*)(a.ws + WS_KM);
    float kmv[32];
#pragma unroll
    for (int i = 0; i < 32; ++i) kmv[i] = kmp[i];
    {
        const __amdgpu_buffer_rsrc_t rQ = mk_rsrc(a.ws + WS_QP, S * D * 2);
        dma16(rQ, d.vk, c * 10240 + wid * 1024, lds0 + K2_Q + wid * 1024);
        if (wid < 2) dma16(rQ, d.vk, c * 10240 + (8 + wid) * 1024, lds0 + K2_Q + (8 + wid) * 1024);
    }
    if (wid < 4) { k2_dma_kv<3>(d, 0, 0); k2_dma_ab<3>(d, 0, 0); } else { k2_dma_kv<2>(d, 0, 0); k2_dma_ab<2>(d, 0, 0); }
    if (wid < 4) { WAIT_VM(5); } else { WAIT_VM(4); }
    BARRIER();
    float km = kmv[0];
#pragma unroll
    for (int i = 1; i < 32; ++i) km = fmaxf(km, kmv[i]);
    bf16x8 q1[2], q2[2];
#pragma unroll
    for (int cb = 0; cb < 2; ++cb) {
        const char* qrow = lds + K2_Q + (rg * 64 + cb * 32 + r) * 40;
        const u32x2 fa = *(const u32x2*)(qrow + 16 * h), fb = *(const u32x2*)(qrow + 16 * h + 8);
        const u32x2 f2 = *(const u32x2*)(qrow + 32);
        u32x4 f1; f1[0] = fa[0]; f1[1] = fa[1]; f1[2] = fb[0]; f1[3] = fb[1];
        float n2 = 0.f;
#pragma unroll
        for (int j = 0; j < 4; ++j) { const float x = __uint_as_float(f1[j] << 16), y = __uint_as_float(f1[j] & 0xffff0000u); n2 += x * x + y * y; }
        if (h == 0) {
#pragma unroll
            for (int j = 0; j < 2; ++j) { const float x = __uint_as_float(f2[j] << 16), y = __uint_as_float(f2[j] & 0xffff0000u); n2 += x * x + y * y; }
        }
        { auto rr = __builtin_amdgcn_permlane32_swap(__float_as_uint(n2), __float_as_uint(n2), false, false); n2 = __uint_as_float(rr[0]) + __uint_as_float(rr[1]); }
        const float m = __builtin_sqrtf(n2 * km) * 1.01f - 100.f;
        u32x4 g2; g2[0] = h ? 0u : f2[0]; g2[1] = h ? 0u : f2[1]; g2[2] = h ? 0u : (pk2(-m, 0.f) & 0xffffu); g2[3] = 0u;
        q1[cb] = __builtin_bit_cast(bf16x8, f1); q2[cb] = __builtin_bit_cast(bf16x8, g2);
    }
    f32x16 o[2], acc[3];
#pragma unroll
    for (int k = 0; k < 16; ++k) { o[0][k] = 0.f; o[1][k] = 0.f; acc[0][k] = 0.f; acc[1][k] = 0.f; acc[2][k] = 0.f; }
    const unsigned sw = (r >> 1) & 7;
    const int dcl = r <= 20 ? r : 21;
    const f32x16 zero = {};
    K2Addr A;
#pragma unroll
    for (int ks = 0; ks < 4; ++ks) {
        A.ga[ks] = lds0 + K2_A + mi * 4096 + r * 128 + (((2 * ks + h) ^ sw) << 4);
        A.gb[ks] = lds0 + K2_B + nj * 3 * 4096 + r * 128 + (((2 * ks + h) ^ sw) << 4);
        PIN(A.ga[ks]); PIN(A.gb[ks]);
    }
    A.kb1 = lds0 + K2_KV + kh * 2 * KV_TILE + h * 512 + r * 16;
    A.kb2 = lds0 + K2_KV + kh * 2 * KV_TILE + (h ? 1536 + 21 * 16 : 1024 + r * 16);
    A.vb = lds0 + K2_KV + kh * 2 * KV_TILE + 1536 + (h * 22 + dcl) * 16;
    PIN(A.kb1); PIN(A.kb2); PIN(A.vb);
    if (wid < 4) { k2_dma_kv<3>(d, 1, 1); k2_dma_ab<3>(d, 1, 1); } else { k2_dma_kv<2>(d, 1, 1); k2_dma_ab<2>(d, 1, 1); }
    if (nj == 0) k2_loop<3>(o, acc, q1, q2, A, d);
    else k2_loop<2>(o, acc, q1, q2, A, d);
    gu32* cnt = (gu32*)(a.ws + WS_CNT);
    {
        f32x4* ex = (f32x4*)(lds + K2_KV + KV_STAGE) + (wid * 3) * 64 + lane;
#pragma unroll
        for (int g = 0; g < 3; ++g) {
            f32x4 t;
#pragma unroll
            for (int j = 0; j < 4; ++j) t[j] = kh ? o[0][4 * g + j] : o[1][4 * g + j];
            ex[g * 64] = t;
        }
    }
    char* ot = lds + K2_KV;
    volatile LDSP int* yflag = (volatile LDSP int*)(uintptr_t)(lds0 + K2_Q);
    if (tid == 0) *yflag = 0;
    WAIT_LGKM0();
    BARRIER();
    {
        const f32x4* exs = (const f32x4*)(lds + K2_KV + KV_STAGE) + ((wid ^ 1) * 3) * 64 + lane;
        float v[12];
#pragma unroll
        for (int g = 0; g < 3; ++g) { const f32x4 t = exs[g * 64];
#pragma unroll
            for (int j = 0; j < 4; ++j) v[4 * g + j] = (kh ? o[1][4 * g + j] : o[0][4 * g + j]) + t[j]; }
        const auto rr = __builtin_amdgcn_permlane32_swap(__float_as_uint(v[8]), __float_as_uint(v[8]), false, false);
        const float l = __uint_as_float(rr[1]);
        const float inv = __builtin_amdgcn_rcpf(l);
        bool bad = !(l >= 7.888609e-31f && l < 3.0e38f);
#pragma unroll
        for (int j = 0; j < 12; ++j) if (j < 8 || h == 0) bad |= !(fabsf(v[j]) < 3.0e38f);
        if (!__any(bad)) {
            char* dst = ot + wid * 1296 + (r * 20 + 4 * h) * 2;
#pragma unroll
            for (int g = 0; g < 3; ++g) {
                if (g < 2 || h == 0) { u32x2 w; w[0] = pk2(v[4 * g] * inv, v[4 * g + 1] * inv); w[1] = pk2(v[4 * g + 2] * inv, v[4 * g + 3] * inv); *(u32x2*)(dst + g * 16) = w; }
            }
        } else {
            const u16* qp = (const u16*)(a.ws + WS_QP);
            const int row = c * 256 + wid * 32 + r;
            const u32x4 f1 = *(const u32x4*)(qp + (size_t)row * 20 + 8 * h);
            const u32x2 f2 = *(const u32x2*)(qp + (size_t)row * 20 + 16);
            u32x4 g2; g2[0] = h ? 0u : f2[0]; g2[1] = h ? 0u : f2[1]; g2[2] = 0u; g2[3] = 0u;
            const bf16x8 fq1 = __builtin_bit_cast(bf16x8, f1), fq2 = __builtin_bit_cast(bf16x8, g2);
            f32x16 oo = {};
            float mrun = -3.0e38f;
            const char* kv = a.ws + WS_KV;
#pragma unroll 1
            for (int T = 0; T < 64; ++T) {
                const char* b = kv + (T >> 2) * KV_STAGE + (T & 3) * KV_TILE;
                const bf16x8 k1 = *(const bf16x8*)(b + h * 512 + r * 16), k2 = *(const bf16x8*)(b + 1024 + r * 16);
                const bf16x8 vf0 = *(const bf16x8*)(b + 1536 + (h * 22 + dcl) * 16), vf1 = *(const bf16x8*)(b + 1536 + ((2 + h) * 22 + dcl) * 16);
                f32x16 s = MFMA32(k1, fq1, zero);
                s = MFMA32(k2, fq2, s);
                float mx = s[0];
#pragma unroll
                for (int j = 1; j < 16; ++j) mx = fmaxf(mx, s[j]);
                mx = fmaxf(mx, __shfl_xor(mx, 32));
                const float mn = fmaxf(mrun, mx);
                const float al = __builtin_amdgcn_exp2f(mrun - mn);
                mrun = mn;
#pragma unroll
                for (int j = 0; j < 16; ++j) oo[j] *= al;
                u32x4 p0, p1;
#pragma unroll
                for (int j = 0; j < 4; ++j) {
                    p0[j] = pk2(__builtin_amdgcn_exp2f(s[2 * j] - mn), __builtin_amdgcn_exp2f(s[2 * j + 1] - mn));
                    p1[j] = pk2(__builtin_amdgcn_exp2f(s[8 + 2 * j] - mn), __builtin_amdgcn_exp2f(s[8 + 2 * j + 1] - mn));
                }
                oo = MFMA32(vf0, __builtin_bit_cast(bf16x8, p0), oo);
                oo = MFMA32(vf1, __builtin_bit_cast(bf16x8, p1), oo);
            }
            const float l2 = __shfl(oo[8], r + 32);
            const float inv2 = 1.0f / l2;
            unsigned* dst = (unsigned*)(ot + wid * 1296 + r * 40);
#pragma unroll
            for (int g = 0; g < 3; ++g) {
                const int d0 = 8 * g + 4 * h;
                if (d0 < 20) { dst[d0 / 2] = pk2(oo[4 * g] * inv2, oo[4 * g + 1] * inv2); dst[d0 / 2 + 1] = pk2(oo[4 * g + 2] * inv2, oo[4 * g + 3] * inv2); }
            }
        }
    }
    WAIT_VM(0);
    WAIT_LGKM0();
    BARRIER();
    if (tid == 0) __hip_atomic_fetch_add(cnt + (32 + mt * 4 + nt) * 32, 1u, __ATOMIC_RELAXED, __HIP_MEMORY_SCOPE_AGENT);
    const __amdgpu_buffer_rsrc_t rO = mk_rsrc(a.ws + WS_OF, S * D * 2);
    {
        const int tb = c >> 1;
#pragma unroll
        for (int p = 0; p < 2; ++p) {
            if (p == 0 || wid < 2) {
                const int i = tid + p * 512;
                const int sq = i & 7, kc = i >> 3;
                const u32x4 v = *(const u32x4*)(ot + sq * 1296 + kc * 16);
                __builtin_amdgcn_raw_buffer_store_b128(v, rO, ((tb * 20 + (kc >> 2)) * 64 + 8 * (c & 1) + sq + 16 * (kc & 3)) * 16, 0, 16);
            }
        }
    }
    WAIT_VM(0);
    BARRIER();
    {
        const int tw = wid & 3, kh2 = wid >> 2;
        const int tb = omt * 4 + tw;
        const __amdgpu_buffer_rsrc_t rC = mk_rsrc(a.ws + WS_CNT, NCNT * 128);
        auto poll = [&](unsigned coff, unsigned need) {
            unsigned spins = 0;
            for (;;) {
                const unsigned v = __builtin_amdgcn_raw_buffer_load_b32(rC, coff, 0, 16);
                if (__builtin_amdgcn_readfirstlane(v) >= need) break;
                __builtin_amdgcn_s_sleep(1);
                if (++spins > (1u << 20)) { if (lane == 0) __hip_atomic_store(cnt + 96 * 32, 1u, __ATOMIC_RELAXED, __HIP_MEMORY_SCOPE_AGENT); break; }
            }
            WAIT_VM(0);
        };
        u32x2 yp[4][3];
        auto yload = [&]() {
#pragma unroll
            for (int p = 0; p < 4; ++p) {
                const __amdgpu_buffer_rsrc_t rY = mk_rsrc(a.ws + WS_YP + (size_t)p * S * D * 2, S * D * 2);
#pragma unroll
                for (int b = 0; b < 3; ++b) { const int eb = ont * 5 + (kh2 ? (b < 2 ? 3 + b : 3) : b); yp[p][b] = __builtin_amdgcn_raw_buffer_load_b64(rY, ((tb * 20 + (eb >> 1)) * 64 + lane) * 16 + (eb & 1) * 8, 0, 16); }
            }
        };
        if (wid == 0) {
            if (lane == 0) __hip_atomic_fetch_add(cnt + (c >> 3) * 32, 1u, __ATOMIC_RELAXED, __HIP_MEMORY_SCOPE_AGENT);
            poll(omt * 128, 8u);
        } else {
            if (wid == 1) { poll((32 + (omt >> 1) * 4 + (ont >> 1)) * 128, 4u); if (lane == 0) *yflag = 1; }
            else { while (*yflag == 0) __builtin_amdgcn_s_sleep(1); }
            SBAR();
            yload();
        }
        const float g = ((const float*)(a.ws + WS_G))[tb * 16 + (lane & 15)];
        BARRIER();
        if (wid == 0) yload();
        bf16x8 of[10];
#pragma unroll
        for (int s = 0; s < 10; ++s) of[s] = __builtin_bit_cast(bf16x8, __builtin_amdgcn_raw_buffer_load_b128(rO, ((tb * 20 + kh2 * 10 + s) * 64 + lane) * 16, 0, 16));
        f32x4 oa[5];
#pragma unroll
        for (int b = 0; b < 5; ++b) oa[b] = f32x4{0.f, 0.f, 0.f, 0.f};
        bf16x8 wf[2][5];
        const unsigned wl = lds0 + lane * 16 + kh2 * 1024;
#pragma unroll
        for (int b = 0; b < 5; ++b) wf[0][b] = lds_ld8(wl + k2_wf_addr(20 * b));
#pragma unroll
        for (int s = 0; s < 10; ++s) {
            if (s + 1 < 10) {
#pragma unroll
                for (int b = 0; b < 5; ++b) wf[(s + 1) & 1][b] = lds_ld8(wl + k2_wf_addr(20 * b + 2 * (s + 1)));
            }
            SBAR();
#pragma unroll
            for (int b = 0; b < 5; ++b) oa[b] = MFMA16(wf[s & 1][b], of[s], oa[b]);
            SBAR();
        }
        {
            f32x4* ex = (f32x4*)(lds + K2_KV + KV_STAGE) + (wid * 3) * 64 + lane;
            if (kh2 == 0) { ex[0] = oa[3]; ex[64] = oa[4]; } else { ex[0] = oa[0]; ex[64] = oa[1]; ex[128] = oa[2]; }
        }
        WAIT_LGKM0();
        BARRIER();
        {
            const f32x4* ex = (const f32x4*)(lds + K2_KV + KV_STAGE) + ((wid ^ 4) * 3) * 64 + lane;
            u16* orow = a.out + (size_t)(omt * 64 + tw * 16 + (lane & 15)) * D + ont * 80 + 4 * (lane >> 4);
#pragma unroll
            for (int b = 0; b < 3; ++b) {
                if (kh2 == 0 || b < 2) {
                    const f32x4 t = ex[b * 64];
                    const f32x4 mine = kh2 ? oa[3 + (b < 2 ? b : 0)] : oa[b];
                    float y[4];
#pragma unroll
                    for (int j = 0; j < 4; ++j) {
                        float sum = 0.f;
#pragma unroll
                        for (int p = 0; p < 4; ++p) sum += (j & 1) ? __uint_as_float(yp[p][b][j >> 1] & 0xffff0000u) : __uint_as_float(yp[p][b][j >> 1] << 16);
                        y[j] = g * (mine[j] + t[j]) + (1.0f - g) * sum;
                    }
                    u32x2 v; v[0] = pk2(y[0], y[1]); v[1] = pk2(y[2], y[3]);
                    *(u32x2*)(orow + 16 * (kh2 * 3 + b)) = v;
                }
            }
        }
    }
}

extern "C" void kernel_launch(void* const* d_in, const int* in_sizes, int n_in, void* d_out, int out_size, void* d_ws, size_t ws_size, hipStream_t stream) {
    (void)in_sizes; (void)n_in; (void)out_size; (void)ws_size;
    (void)hipFuncSetAttribute((const void*)k1_proj, hipFuncAttributeMaxDynamicSharedMemorySize, K1_LDS);
    (void)hipFuncSetAttribute((const void*)k2_main, hipFuncAttributeMaxDynamicSharedMemorySize, K2_LDS);
    K1Args a1;
    a1.xa = (const u16*)d_in[0]; a1.wq = (const u16*)d_in[1]; a1.wk = (const u16*)d_in[2]; a1.wv = (const u16*)d_in[3]; a1.wo = (const u16*)d_in[4];
    a1.wr = (const u16*)d_in[8]; a1.wi = (const u16*)d_in[9]; a1.gw = (const u16*)d_in[10];
    a1.cre = (const u16*)d_in[6]; a1.cim = (const u16*)d_in[7];
    a1.ws = (char*)d_ws;
        hipLaunchKernelGGL(k1_proj, dim3(256), dim3(512), K1_LDS, stream, a1);
    K2Args a2; a2.cre = (const u16*)d_in[6]; a2.cim = (const u16*)d_in[7]; a2.ws = (char*)d_ws; a2.out = (u16*)d_out;
    hipLaunchKernelGGL(k2_main, dim3(256), dim3(512), K2_LDS, stream, a2);
}
```
